```python
import torch

def _softmax(x, axis=-1):
    return torch.softmax(x, dim=axis)
def _gelu(x):
    return torch.nn.functional.gelu(x, approximate="tanh")
def _silu(x):
    return torch.nn.functional.silu(x)
def _sigmoid(x):
    return torch.sigmoid(x)
def _layernorm(x, axis=-1, eps=1e-5):
    m = x.mean(dim=axis, keepdim=True)
    v = x.var(dim=axis, keepdim=True, unbiased=False)
    return (x - m) / torch.sqrt(v + eps)
def _scatter_add(tgt, idx, src, axis=0):
    return tgt.clone().index_add_(axis, idx, src)
def _segment_sum(src, seg, n):
    out = torch.zeros((n,) + src.shape[1:], dtype=src.dtype)
    return out.index_add_(0, seg, src)
def _pad(x, pw):
    flat = tuple(v for pair in reversed(pw) for v in pair)
    return torch.nn.functional.pad(x, flat)

kernel_name = 'sequential_preLN__mha_block__swiglu_ffn__prompt'

def setup_inputs():

    _w = (lambda a: (lambda t: t.to(torch.bfloat16) if t.is_floating_point() else t)(torch.as_tensor(a)))
    def _A_setup():
        B, S, D, H = 1, 2048, 640, 32
        return {
            "x": _w((lambda *s: torch.randn(*s, dtype=torch.bfloat16) * 0.05)(B, S, D)),
            "wqkv": _w((lambda *s: torch.randn(*s, dtype=torch.bfloat16) * 0.05)(D, 3 * D)),
            "wo": _w((lambda *s: torch.randn(*s, dtype=torch.bfloat16) * 0.05)(D, D)),
        }
    _A_in = _A_setup()

    def _B_setup():
        B, S, D, F = 1, 2048, 640, 1728
        return {
            "x": _w((lambda *s: torch.randn(*s, dtype=torch.bfloat16) * 0.05)(B, S, D)),
            "w_gate": _w((lambda *s: torch.randn(*s, dtype=torch.bfloat16) * 0.05)(D, F)),
            "w_up": _w((lambda *s: torch.randn(*s, dtype=torch.bfloat16) * 0.05)(D, F)),
            "w_down": _w((lambda *s: torch.randn(*s, dtype=torch.bfloat16) * 0.05)(F, D)),
        }
    _B_in = _B_setup()

    return {
        "A_x": _A_in["x"],
        "A_wqkv": _A_in["wqkv"],
        "A_wo": _A_in["wo"],
        "B_x": _B_in["x"],
        "B_w_gate": _B_in["w_gate"],
        "B_w_up": _B_in["w_up"],
        "B_w_down": _B_in["w_down"],
    }

def reference(A_x, A_wqkv, A_wo, B_x, B_w_gate, B_w_up, B_w_down):
    def _A_ref(x, wqkv, wo):
        B, S, D = x.shape
        H = 32
        Dh = D // H
        qkv = (lambda x, *d: x.permute(*d))((x @ wqkv).reshape(B, S, 3, H, Dh), 2, 0, 3, 1, 4)
        q, k, v = qkv[0], qkv[1], qkv[2]
        scores = torch.einsum("bhqd,bhkd->bhqk", q, k) * (Dh ** -0.5)
        p = _softmax(scores, axis=-1)
        o = torch.einsum("bhqk,bhkd->bhqd", p, v)
        return (lambda x, *d: x.permute(*d))(o, 0, 2, 1, 3).reshape(B, S, D) @ wo

    def _B_ref(x, w_gate, w_up, w_down):
        g = _silu(x @ w_gate)
        u = x @ w_up
        return (g * u) @ w_down

    x = A_x
    h = x + _A_ref(_layernorm(x, axis=-1), A_wqkv, A_wo)
    h2 = h + _B_ref(_layernorm(h, axis=-1), B_w_gate, B_w_up, B_w_down)
    return h2
```

```cpp
#include <hip/hip_runtime.h>
#include <stdint.h>

typedef __attribute__((ext_vector_type(8))) short s16x8;
typedef __attribute__((ext_vector_type(4))) short s16x4;
typedef __attribute__((ext_vector_type(2))) __bf16 bf16x2;
typedef __attribute__((ext_vector_type(2))) float f32x2;
typedef __attribute__((ext_vector_type(4))) float f32x4;
typedef __attribute__((ext_vector_type(16))) float f32x16;
typedef __attribute__((ext_vector_type(2))) unsigned u32x2;
typedef __attribute__((ext_vector_type(4))) unsigned u32x4;
#define LDS_AS __attribute__((address_space(3)))
#define DEV __device__ __forceinline__

constexpr int S_ = 2048, D_ = 640, F_ = 1728;
constexpr int KVP = 21504;
constexpr int QP = 10240;
constexpr size_t OFF_QB = 0;
constexpr size_t OFF_KV = OFF_QB + (size_t)32 * 8 * QP;
constexpr size_t OFF_OB = OFF_KV + (size_t)32 * 8 * KVP;
constexpr size_t OFF_HB = OFF_OB + (size_t)128 * 20 * 1024;
constexpr size_t OFF_MB = OFF_HB + (size_t)128 * 20 * 1024;
constexpr size_t OFF_WO = OFF_MB + (size_t)128 * 54 * 1024;
constexpr size_t OFF_WGU = OFF_WO + (size_t)8 * 20 * 5 * 1024;
constexpr size_t OFF_WD = OFF_WGU + (size_t)16 * 20 * 14 * 1024;
constexpr size_t OFF_CSP = OFF_WD + (size_t)8 * 54 * 5 * 1024 + 16384;
constexpr size_t OFF_ST = OFF_CSP + (size_t)5 * 3584 * 4;
constexpr size_t WS_END = OFF_ST + (size_t)2048 * 8 * 8;

#define MFMA32(a, b, c) __builtin_amdgcn_mfma_f32_32x32x16_bf16((a), (b), (c), 0, 0, 0)
#define MFMA32H(a, b, c) __builtin_amdgcn_mfma_f32_32x32x8bf16_1k((a), (b), (c), 0, 0, 0)
#define MFMA16(a, b, c) __builtin_amdgcn_mfma_f32_16x16x32_bf16((a), (b), (c), 0, 0, 0)

DEV void glds(const void* sbase, unsigned voff, unsigned lds) {
  unsigned keep;
  asm volatile("s_mov_b32 %0, m0\n\ts_mov_b32 m0, %3\n\ts_nop 0\n\tglobal_load_lds_dwordx4 %1, %2\n\ts_mov_b32 m0, %0"
               : "=&s"(keep) : "v"(voff), "s"(sbase), "s"(lds) : "memory");
}
template <int N> DEV void waitv() { asm volatile("s_waitcnt vmcnt(%0)" ::"i"(N) : "memory"); }
DEV void bar() { asm volatile("s_barrier" ::: "memory"); }
DEV void lbar() { asm volatile("s_waitcnt lgkmcnt(0)\n\ts_barrier" ::: "memory"); }
DEV unsigned pk2(float a, float b) { f32x2 v = {a, b}; return __builtin_bit_cast(unsigned, __builtin_convertvector(v, bf16x2)); }
DEV float dot2(unsigned a, unsigned b, float c) {
  return __builtin_amdgcn_fdot2_f32_bf16(__builtin_bit_cast(bf16x2, a), __builtin_bit_cast(bf16x2, b), c, false);
}
DEV float bflo(unsigned v) { return __uint_as_float(v << 16); }
DEV float bfhi(unsigned v) { return __uint_as_float(v & 0xffff0000u); }
DEV float xsum32(float x) {
  auto r = __builtin_amdgcn_permlane32_swap(__float_as_uint(x), __float_as_uint(x), false, false);
  return __uint_as_float(r[0]) + __uint_as_float(r[1]);
}
DEV float xsum16(float x) {
  auto r = __builtin_amdgcn_permlane16_swap(__float_as_uint(x), __float_as_uint(x), false, false);
  return __uint_as_float(r[0]) + __uint_as_float(r[1]);
}
DEV float xmax32(float x) {
  auto r = __builtin_amdgcn_permlane32_swap(__float_as_uint(x), __float_as_uint(x), false, false);
  return __builtin_fmaxf(__uint_as_float(r[0]), __uint_as_float(r[1]));
}
template <class T> DEV T ldsr(unsigned a) { return *(LDS_AS T*)(size_t)a; }
template <class T> DEV void ldsw(unsigned a, T v) { *(LDS_AS T*)(size_t)a = v; }
DEV s16x4 ldstr(unsigned a) { return __builtin_amdgcn_ds_read_tr16_b64_v4i16((LDS_AS s16x4*)(size_t)a); }
template <int I> struct IC { static constexpr int v = I; };
template <int B, int E, class Fn> DEV void sfor(Fn&& f) {
  if constexpr (B < E) { f(IC<B>{}); sfor<B + 1, E>(f); }
}

DEV void prep_task(int T, const unsigned short* wo, const unsigned short* wg, const unsigned short* wu,
                   const unsigned short* wd, char* ws, int lane) {
  const unsigned short* src; int ld, col0, kb0, nkb = 4, dstride; char* dst; float* cs = nullptr;
  if (T < 200) {
    const int kg = T / 40, nt = T % 40;
    src = wo; ld = D_; col0 = 16 * nt; kb0 = 4 * kg; dstride = 5 * 1024;
    dst = ws + OFF_WO + (size_t)((nt / 5) * 20 * 5 + (nt % 5)) * 1024;
  } else if (T < 1320) {
    const int U = T - 200, kg = U / 224, r = U % 224, gu = r / 112, vt = r % 112;
    const int ft = vt < 105 ? vt : vt - 4;
    src = gu ? wu : wg; ld = F_; col0 = 16 * ft; kb0 = 4 * kg; dstride = 14 * 1024;
    dst = ws + OFF_WGU + (size_t)((vt / 7) * 20 * 14 + (vt % 7) * 2 + gu) * 1024;
    cs = (float*)(ws + OFF_CSP) + kg * 3584 + (vt * 2 + gu) * 16;
  } else {
    const int U = T - 1320, kg = U / 40, nt = U % 40;
    src = wd; ld = D_; col0 = 16 * nt; kb0 = 4 * kg; nkb = kg == 13 ? 2 : 4; dstride = 5 * 1024;
    dst = ws + OFF_WD + (size_t)((nt / 5) * 54 * 5 + (nt % 5)) * 1024;
  }
  const int g = lane >> 4, n = lane & 15;
  const unsigned short* p = src + (size_t)(32 * kb0 + 4 * g) * ld + col0 + n;
  float acc = 0.f;
#pragma unroll
  for (int b = 0; b < 4; ++b) {
    if (b < nkb) {
      u32x4 v;
#pragma unroll
      for (int q = 0; q < 4; ++q) {
        const int r0 = 32 * b + 16 * (q >> 1) + 2 * (q & 1);
        const unsigned lo = p[(size_t)r0 * ld], hi = p[(size_t)(r0 + 1) * ld];
        v[q] = lo | (hi << 16);
        acc = dot2(v[q], 0x3f803f80u, acc);
      }
      *(u32x4*)(dst + (size_t)(kb0 + b) * dstride + lane * 16) = v;
    }
  }
  if (cs) {
    acc = xsum32(xsum16(acc));
    if (lane < 16) cs[lane] = acc;
  }
}
__global__ __launch_bounds__(512) void k_prep(const unsigned short* wo, const unsigned short* wg,
                                              const unsigned short* wu, const unsigned short* wd, char* ws) {
  const int T = blockIdx.x * 8 + __builtin_amdgcn_readfirstlane(threadIdx.x >> 6);
  if (T < 1880) prep_task(T, wo, wg, wu, wd, ws, threadIdx.x & 63);
}

template <bool ISV>
DEV void qkv_compute(unsigned l0, int w, int lane, int rt, int ct, char* ws) {
  const int wm = w >> 1, wn = w & 1, r = lane & 31, h = lane >> 5;
  f32x16 acc[2][2];
#pragma unroll
  for (int i = 0; i < 2; ++i)
#pragma unroll
    for (int j = 0; j < 2; ++j)
#pragma unroll
      for (int e = 0; e < 16; ++e) acc[i][j][e] = 0.f;
  float sx = 0.f, sxx = 0.f, cs = 0.f;
  const unsigned xo = (wm * 64 + r) * 128 + ((h ^ ((r >> 1) & 7)) << 4);
  const unsigned xo_[2] = {xo + wn * 4096, xo + (wn ^ 1) * 4096};
  unsigned wo_[2][2];
  {
    const int blk = (lane >> 4) & 1, q = (lane & 15) >> 2, p = lane & 3;
#pragma unroll
    for (int j = 0; j < 2; ++j)
#pragma unroll
      for (int t = 0; t < 2; ++t) {
        const int row = 8 * h + 4 * t + q, ch = 4 * (2 * wn + (j ^ wm)) + 2 * blk + (p >> 1);
        wo_[j][t] = 16384 + 256 * row + 16 * (ch ^ ((q << 2) | (2 * h + t))) + 8 * (p & 1);
      }
  }
  for (int s = 0; s < 10; ++s) {
    lbar();
    const unsigned sb = l0 + (s & 3) * 32768;
#pragma unroll
    for (int ks = 0; ks < 4; ++ks) {
      s16x8 xf[2], wf[2];
#pragma unroll
      for (int i = 0; i < 2; ++i) xf[i] = ldsr<s16x8>(sb + (xo_[i] ^ (ks << 5)));
#pragma unroll
      for (int j = 0; j < 2; ++j) {
        const s16x4 a = ldstr(sb + wo_[j][0] + ks * 4096), b = ldstr(sb + wo_[j][1] + ks * 4096);
        wf[j] = __builtin_shufflevector(a, b, 0, 1, 2, 3, 4, 5, 6, 7);
      }
#pragma unroll
      for (int i = 0; i < 2; ++i)
#pragma unroll
        for (int j = 0; j < 2; ++j) acc[i][j] = ISV ? MFMA32(xf[i], wf[j], acc[i][j]) : MFMA32(wf[j], xf[i], acc[i][j]);
      const u32x4 xs = __builtin_bit_cast(u32x4, xf[0]);
      const u32x4 wsv = __builtin_bit_cast(u32x4, wf[0]);
#pragma unroll
      for (int e = 0; e < 4; ++e) {
        sx = dot2(xs[e], 0x3f803f80u, sx);
        sxx = dot2(xs[e], xs[e], sxx);
        cs = dot2(wsv[e], 0x3f803f80u, cs);
      }
    }
  }
  sx = xsum32(sx); sxx = xsum32(sxx); cs = xsum32(cs);
  const unsigned stb = l0 + 131072, csb = stb + 1024;
  {
    const float mean = sx * (1.f / D_);
    const float var = __builtin_fmaxf(sxx * (1.f / D_) - mean * mean, 0.f);
    float a = 1.f / __builtin_sqrtf(var + 1e-5f);
    if (ct < 5) a *= 0.22360679774997896f * 1.4426950408889634f;
    if (h == 0) ldsw<f32x2>(stb + (wm * 64 + wn * 32 + r) * 8, f32x2{a, -mean * a});
    else ldsw<float>(csb + (wn * 64 + wm * 32 + r) * 4, cs);
  }
  lbar();
  const int grp = rt >> 1;
  if constexpr (!ISV) {
    const bool isq = ct < 5;
    char* base = ws + (isq ? OFF_QB : OFF_KV) + (size_t)grp * (isq ? QP : KVP);
    const unsigned hp = 8 * (isq ? QP : KVP);
    const int c0 = (isq ? ct : ct - 5) * 32 + wn * 16 + h;
#pragma unroll
    for (int i = 0; i < 2; ++i) {
      const f32x2 st = ldsr<f32x2>(stb + (wm * 64 + (i ^ wn) * 32 + r) * 8);
      const int tile = (rt & 1) * 4 + wm * 2 + (i ^ wn);
#pragma unroll
      for (int j = 0; j < 2; ++j)
#pragma unroll
        for (int u = 0; u < 4; ++u) {
          const f32x4 c4 = ldsr<f32x4>(csb + (wn * 64 + (j ^ wm) * 32 + 8 * u + 4 * h) * 4);
          const int Qi = c0 + (j ^ wm) * 8 + 2 * u;
          const int head = (Qi * 52429) >> 18, qd = Qi - 5 * head;
          const unsigned off = head * hp + (qd < 4 ? tile * 1024 + (qd >> 1) * 512 + r * 16 + (qd & 1) * 8
                                                   : 8192 + (tile * 32 + r) * 8);
          u32x2 o;
          o[0] = pk2(st[0] * acc[i][j][4 * u] + st[1] * c4[0], st[0] * acc[i][j][4 * u + 1] + st[1] * c4[1]);
          o[1] = pk2(st[0] * acc[i][j][4 * u + 2] + st[1] * c4[2], st[0] * acc[i][j][4 * u + 3] + st[1] * c4[3]);
          *(u32x2*)(base + off) = o;
        }
    }
  } else {
    char* base = ws + OFF_KV + (size_t)grp * KVP + 10240;
#pragma unroll
    for (int j = 0; j < 2; ++j) {
      const float c1 = ldsr<float>(csb + (wn * 64 + (j ^ wm) * 32 + r) * 4);
      const int cc = (ct - 10) * 128 + wn * 64 + (j ^ wm) * 32 + r;
      const int head = (cc * 52429) >> 20, dim = cc - 20 * head;
#pragma unroll
      for (int i = 0; i < 2; ++i) {
        const int tile = (rt & 1) * 4 + wm * 2 + (i ^ wn);
#pragma unroll
        for (int s = 0; s < 2; ++s) {
          u32x4 o;
#pragma unroll
          for (int u = 0; u < 2; ++u) {
            const unsigned sa = stb + (wm * 64 + (i ^ wn) * 32 + 16 * s + 8 * u + 4 * h) * 8;
            const f32x4 s0 = ldsr<f32x4>(sa), s1 = ldsr<f32x4>(sa + 16);
            const int e = 8 * s + 4 * u;
            o[2 * u] = pk2(s0[0] * acc[i][j][e] + s0[1] * c1, s0[2] * acc[i][j][e + 1] + s0[3] * c1);
            o[2 * u + 1] = pk2(s1[0] * acc[i][j][e + 2] + s1[1] * c1, s1[2] * acc[i][j][e + 3] + s1[3] * c1);
          }
          char* p = base + (size_t)head * (8 * KVP) + ((tile * 2 + s) * 2 + h) * 336 + dim * 16;
          *(u32x4*)p = o;
          if (dim == 19) *(u32x4*)(p + 16) = u32x4{0x3f803f80u, 0x3f803f80u, 0x3f803f80u, 0x3f803f80u};
        }
      }
    }
  }
}

__global__ __launch_bounds__(512) void k_qkv(const char* x, const char* wqkv, char* ws) {
  __shared__ __attribute__((aligned(16))) char lds[131072 + 1024 + 512];
  const int bid = blockIdx.x, xcd = bid & 7, loc = bid >> 3;
  const int rt = 4 * (xcd >> 1) + (loc & 3), ct = 8 * (xcd & 1) + (loc >> 2);
  if (ct == 15) return;
  const int lane = threadIdx.x & 63, w = __builtin_amdgcn_readfirstlane(threadIdx.x >> 6);
  const unsigned l0 = (unsigned)(size_t)(LDS_AS char*)lds;
  if (w >= 4) {
    const int i = w - 4;
    unsigned xo[4], wo[4];
#pragma unroll
    for (int j = 0; j < 4; ++j) {
      const int p = 4 * i + j;
      const int xr = 8 * p + (lane >> 3);
      xo[j] = xr * 1280 + (((lane & 7) ^ ((xr >> 1) & 7)) << 4);
      const int wr = 4 * p + (lane >> 4);
      wo[j] = wr * 3840 + (((lane & 15) ^ (((lane >> 4) << 2) | j)) << 4);
    }
    const char* xb = x + (size_t)rt * 128 * 1280;
    const char* wb = wqkv + ct * 256;
    const unsigned lb = l0 + i * 4096;
    auto issue = [&](int s) {
      const unsigned sb = lb + (s & 3) * 32768;
#pragma unroll
      for (int j = 0; j < 4; ++j) glds(xb + s * 128, xo[j], sb + j * 1024);
#pragma unroll
      for (int j = 0; j < 4; ++j) glds(wb + (size_t)s * (64 * 3840), wo[j], sb + 16384 + j * 1024);
    };
    issue(0); issue(1); issue(2);
    for (int s = 0; s < 7; ++s) { waitv<16>(); bar(); issue(s + 3); }
    waitv<16>(); bar();
    waitv<8>(); bar();
    waitv<0>(); bar();
    return;
  }
  if (ct < 10) qkv_compute<false>(l0, w, lane, rt, ct, ws);
  else qkv_compute<true>(l0, w, lane, rt, ct, ws);
}

DEV void attn_exp(const f32x16& s, s16x8& p0, s16x8& p1) {
  u32x4 a, b;
#pragma unroll
  for (int e = 0; e < 4; ++e) {
    a[e] = pk2(__builtin_amdgcn_exp2f(s[2 * e]), __builtin_amdgcn_exp2f(s[2 * e + 1]));
    b[e] = pk2(__builtin_amdgcn_exp2f(s[8 + 2 * e]), __builtin_amdgcn_exp2f(s[8 + 2 * e + 1]));
  }
  p0 = __builtin_bit_cast(s16x8, a); p1 = __builtin_bit_cast(s16x8, b);
}
DEV float max16(const f32x16& s) {
  float m = __builtin_fmaxf(s[0], s[1]);
#pragma unroll
  for (int e = 2; e < 16; ++e) m = __builtin_fmaxf(m, s[e]);
  return xmax32(m);
}

__global__ __launch_bounds__(512) void k_attn(char* ws) {
  __shared__ __attribute__((aligned(16))) char lds[7 * KVP + QP];
  const int bid = blockIdx.x, head = 4 * (bid & 7) + ((bid >> 3) & 3), qg = bid >> 5;
  const int lane = threadIdx.x & 63, w = __builtin_amdgcn_readfirstlane(threadIdx.x >> 6);
  const int r = lane & 31, h = lane >> 5;
  const unsigned l0 = (unsigned)(size_t)(LDS_AS char*)lds;
  const char* kv = ws + OFF_KV + (size_t)head * (8 * KVP);
  const char* qb = ws + OFF_QB + (size_t)(head * 8 + qg) * QP;
  const int p2 = w < 5 ? w + 16 : 20;
  const unsigned v0 = w * 1024 + lane * 16, v2 = p2 * 1024 + lane * 16;
  auto issue = [&](int c, int slot) {
    const char* b = kv + c * KVP; const unsigned sb = l0 + slot * KVP;
    glds(b, v0, sb + w * 1024); glds(b + 8192, v0, sb + 8192 + w * 1024); glds(b, v2, sb + p2 * 1024);
  };
  glds(qb, v0, l0 + 7 * KVP + w * 1024);
  glds(qb + 8192, (w & 1) * 1024 + lane * 16, l0 + 7 * KVP + 8192 + (w & 1) * 1024);
#pragma unroll
  for (int c = 0; c < 7; ++c) issue(c, c);
  const unsigned ka = l0 + lane * 16, kha = l0 + 8192 + r * 8;
  const unsigned va = l0 + 10240 + h * 336 + (r <= 20 ? r : (r < 28 ? 20 : 4)) * 16;
  waitv<18>(); bar();
  const s16x8 qlo = ldsr<s16x8>(l0 + 7 * KVP + w * 1024 + lane * 16);
  s16x4 qhi = ldsr<s16x4>(l0 + 7 * KVP + 8192 + (w * 32 + r) * 8);
  if (h) qhi = s16x4{0, 0, 0, 0};
  f32x16 z;
#pragma unroll
  for (int e = 0; e < 16; ++e) z[e] = 0.f;
  f32x16 sc = MFMA32(ldsr<s16x8>(ka), qlo, MFMA32H(ldsr<s16x4>(kha), qhi, z));
  const float m0 = max16(sc);
  f32x16 negm, o = z;
#pragma unroll
  for (int e = 0; e < 16; ++e) { negm[e] = -m0; sc[e] -= m0; }
  s16x8 kl = ldsr<s16x8>(ka + 1024);
  s16x4 kh = ldsr<s16x4>(kha + 256);
  s16x8 vA = ldsr<s16x8>(va), vB = ldsr<s16x8>(va + 672);
  unsigned so = 0;
  for (int c = 0; c < 8; ++c) {
    const unsigned sn = c == 6 ? 0 : so + KVP;
#pragma unroll
    for (int t = 0; t < 8; ++t) {
      if (t == 4) {
        if (c == 0) { waitv<12>(); bar(); }
        else if (c == 1) { waitv<6>(); lbar(); issue(7, 0); }
        else if (c == 3) { waitv<0>(); bar(); }
      }
      const f32x16 sx = MFMA32(kl, qlo, MFMA32H(kh, qhi, negm));
      const unsigned k2 = t < 6 ? so + (t + 2) * 1024 : sn + (t - 6) * 1024;
      const unsigned h2 = t < 6 ? so + (t + 2) * 256 : sn + (t - 6) * 256;
      kl = ldsr<s16x8>(ka + k2); kh = ldsr<s16x4>(kha + h2);
      s16x8 p0, p1;
      attn_exp(sc, p0, p1);
      o = MFMA32(vA, p0, o);
      o = MFMA32(vB, p1, o);
      const unsigned v1 = t < 7 ? so + (t + 1) * 1344 : sn;
      vA = ldsr<s16x8>(va + v1); vB = ldsr<s16x8>(va + v1 + 672);
      sc = sx;
    }
    so = sn;
  }
  auto lsw = __builtin_amdgcn_permlane32_swap(__float_as_uint(o[8]), __float_as_uint(o[8]), false, false);
  float l = __uint_as_float(lsw[1]);
  {
    bool bad = !(l < 1.8446744e19f);
#pragma unroll
    for (int e = 0; e < 12; ++e) bad = bad || !(__builtin_fabsf(o[e]) < 3.0e38f);
    if (__builtin_amdgcn_ballot_w64(bad) != 0) {
      float m = -__builtin_inff();
      o = z;
      const unsigned gv = 10240 + h * 336 + (r <= 20 ? r : 20) * 16;
      for (int t = 0; t < 64; ++t) {
        const char* b = kv + (t >> 3) * KVP; const int tl = t & 7;
        f32x16 s = MFMA32(*(const s16x8*)(b + tl * 1024 + lane * 16), qlo,
                          MFMA32H(*(const s16x4*)(b + 8192 + tl * 256 + r * 8), qhi, z));
        const float mn = __builtin_fmaxf(m, max16(s));
        const float al = __builtin_amdgcn_exp2f(m - mn);
        m = mn;
#pragma unroll
        for (int e = 0; e < 16; ++e) { s[e] -= mn; o[e] *= al; }
        s16x8 p0, p1;
        attn_exp(s, p0, p1);
        o = MFMA32(*(const s16x8*)(b + gv + tl * 1344), p0, o);
        o = MFMA32(*(const s16x8*)(b + gv + tl * 1344 + 672), p1, o);
      }
      lsw = __builtin_amdgcn_permlane32_swap(__float_as_uint(o[8]), __float_as_uint(o[8]), false, false);
      l = __uint_as_float(lsw[1]);
    }
  }
  const float inv = 1.f / l;
  const int tok = qg * 256 + w * 32 + r;
  char* ob = ws + OFF_OB + (size_t)(tok >> 4) * 20480 + (tok & 15) * 16;
#pragma unroll
  for (int u = 0; u < 3; ++u) {
    if (u < 2 || h == 0) {
      const int f = head * 20 + 8 * u + 4 * h;
      u32x2 v;
      v[0] = pk2(o[4 * u] * inv, o[4 * u + 1] * inv);
      v[1] = pk2(o[4 * u + 2] * inv, o[4 * u + 3] * inv);
      *(u32x2*)(ob + (f >> 5) * 1024 + ((f >> 2) & 3) * 256 + ((f >> 4) & 1) * 8) = v;
    }
  }
}

template <int MODE> struct GC;
template <> struct GC<0> { static constexpr int TT = 1, NT = 5, KB = 20, KPS = 4, SLOTS = 5, XD = 20, UK = 2, UN = 5; };
template <> struct GC<1> { static constexpr int TT = 2, NT = 14, KB = 20, KPS = 2, SLOTS = 5, XD = 4, UK = 1, UN = 7; };
template <> struct GC<2> { static constexpr int TT = 1, NT = 5, KB = 54, KPS = 4, SLOTS = 7, XD = 16, UK = 2, UN = 5; };

template <int MODE>
__global__ __launch_bounds__(512) void k_gemm(const char* xin, char* ws, char* out) {
  using C = GC<MODE>;
  constexpr int TT = C::TT, NT = C::NT, KB = C::KB, KPS = C::KPS, SLOTS = C::SLOTS, XD = C::XD, UK = C::UK, UN = C::UN;
  constexpr int NS = (KB + KPS - 1) / KPS, SLOT = NT * KPS * 1024, PER = NT * KPS / 4;
  constexpr int UPK = NT / UN;
  constexpr int NU = (KB / UK) * UPK;
  constexpr int UPS = (KPS / UK) * UPK;
  __shared__ __attribute__((aligned(16))) char lds[SLOTS * SLOT + 1024];
  const int bid = blockIdx.x, xcd = bid & 7, loc = bid >> 3;
  int tb, cg;
  if constexpr (MODE == 1) { tb = 4 * (xcd >> 1) + (loc & 3); cg = 8 * (xcd & 1) + (loc >> 2); }
  else { tb = 8 * (xcd >> 1) + (loc & 7); cg = 4 * (xcd & 1) + (loc >> 3); }
  const int lane = threadIdx.x & 63, w = __builtin_amdgcn_readfirstlane(threadIdx.x >> 6);
  const unsigned l0 = (unsigned)(size_t)(LDS_AS char*)lds;
  if (w >= 4) {
    const char* wb = ws + (MODE == 0 ? OFF_WO : MODE == 1 ? OFF_WGU : OFF_WD) + (size_t)cg * (KB * NT * 1024);
    const unsigned vo = (w - 4) * (PER * 1024) + lane * 16, lb = l0 + (w - 4) * (PER * 1024);
    constexpr int PRE = SLOTS >= NS ? NS : SLOTS;
    auto issue = [&](int s) {
#pragma unroll
      for (int j = 0; j < PER; ++j) glds(wb + (size_t)s * SLOT + j * 1024, vo, lb + (s % SLOTS) * SLOT + j * 1024);
    };
    sfor<0, PRE>([&](auto I) { issue(I.v); });
    sfor<0, NS>([&](auto I) {
      constexpr int s = I.v, i0 = SLOTS + (s >= 1 ? s - 1 : 0), issued = i0 < NS ? i0 : NS;
      waitv<(issued - s - 1) * PER>();
      bar();
      if constexpr (s >= 1 && s - 1 + SLOTS < NS) issue(s - 1 + SLOTS);
    });
    return;
  }
  const int tok16 = lane & 15, g = lane >> 4;
  const int tt0 = (tb * 4 + w) * TT;
  const char* xb = ws + (MODE == 0 ? OFF_OB : MODE == 1 ? OFF_HB : OFF_MB) + (size_t)tt0 * (KB * 1024) + lane * 16;
  s16x8 xr[XD][TT];
  sfor<0, XD>([&](auto I) {
#pragma unroll
    for (int t = 0; t < TT; ++t) xr[I.v][t] = *(const s16x8*)(xb + (size_t)t * (KB * 1024) + I.v * 1024);
  });
  u32x2 res[NT];
  float sa[TT], sb_[TT];
  if constexpr (MODE == 0) {
#pragma unroll
    for (int n = 0; n < NT; ++n) res[n] = *(const u32x2*)(xin + (size_t)(tt0 * 16 + tok16) * 1280 + (cg * 80 + n * 16 + 4 * g) * 2);
  } else if constexpr (MODE == 2) {
#pragma unroll
    for (int n = 0; n < NT; ++n) {
      const int nt = cg * 5 + n;
      res[n] = *(const u32x2*)(ws + OFF_HB + (size_t)(tt0 * 20 + (nt >> 1)) * 1024 + lane * 16 + (nt & 1) * 8);
    }
  } else {
#pragma unroll
    for (int t = 0; t < TT; ++t) {
      const f32x4 p = *(const f32x4*)(ws + OFF_ST + (size_t)((tt0 + t) * 16 + tok16) * 64 + g * 16);
      const float s1 = xsum32(xsum16(p[0] + p[2])), s2 = xsum32(xsum16(p[1] + p[3]));
      const float mean = s1 * (1.f / D_);
      const float var = __builtin_fmaxf(s2 * (1.f / D_) - mean * mean, 0.f) + 1e-5f;
      sa[t] = -mean; sb_[t] = var;
    }
    if (w == 0 && lane < 56) {
      const float* cp = (const float*)(ws + OFF_CSP) + cg * 224 + lane * 4;
      f32x4 c = *(const f32x4*)cp;
#pragma unroll
      for (int k = 1; k < 5; ++k) c += *(const f32x4*)(cp + k * 3584);
      ldsw<f32x4>(l0 + SLOTS * SLOT + lane * 16, c);
    }
  }
  f32x4 acc[TT][NT];
#pragma unroll
  for (int t = 0; t < TT; ++t)
#pragma unroll
    for (int n = 0; n < NT; ++n) acc[t][n] = f32x4{0.f, 0.f, 0.f, 0.f};
  s16x8 wf[2][UK][UN];
  const unsigned la = l0 + lane * 16;
  auto rd = [&](auto U) {
    constexpr int u = U.v, kb0 = (u / UPK) * UK, n0 = (u % UPK) * UN;
#pragma unroll
    for (int k = 0; k < UK; ++k)
#pragma unroll
      for (int n = 0; n < UN; ++n) {
        const int kb = kb0 + k;
        wf[u & 1][k][n] = ldsr<s16x8>(la + ((kb / KPS) % SLOTS) * SLOT + ((kb % KPS) * NT + n0 + n) * 1024);
      }
  };
  lbar();
  rd(IC<0>{});
  sfor<0, NU>([&](auto U) {
    constexpr int u = U.v, kb0 = (u / UPK) * UK, n0 = (u % UPK) * UN;
    if constexpr (u + 1 < NU) {
      if constexpr ((u + 1) % UPS == 0) lbar();
      rd(IC<u + 1>{});
    }
#pragma unroll
    for (int k = 0; k < UK; ++k) {
      const int kb = kb0 + k;
#pragma unroll
      for (int n = 0; n < UN; ++n)
#pragma unroll
        for (int t = 0; t < TT; ++t) acc[t][n0 + n] = MFMA16(wf[u & 1][k][n], xr[kb % XD][t], acc[t][n0 + n]);
      if ((u % UPK) == UPK - 1 && kb + XD < KB) {
#pragma unroll
        for (int t = 0; t < TT; ++t) xr[kb % XD][t] = *(const s16x8*)(xb + (size_t)t * (KB * 1024) + (kb + XD) * 1024);
      }
    }
  });
  if constexpr (MODE == 0) {
    float s1 = 0.f, s2 = 0.f;
#pragma unroll
    for (int n = 0; n < NT; ++n) {
      u32x2 v;
      v[0] = pk2(acc[0][n][0] + bflo(res[n][0]), acc[0][n][1] + bfhi(res[n][0]));
      v[1] = pk2(acc[0][n][2] + bflo(res[n][1]), acc[0][n][3] + bfhi(res[n][1]));
      s1 = dot2(v[0], 0x3f803f80u, s1); s1 = dot2(v[1], 0x3f803f80u, s1);
      s2 = dot2(v[0], v[0], s2); s2 = dot2(v[1], v[1], s2);
      const int nt = cg * 5 + n;
      *(u32x2*)(ws + OFF_HB + (size_t)(tt0 * 20 + (nt >> 1)) * 1024 + lane * 16 + (nt & 1) * 8) = v;
    }
    s1 = xsum32(xsum16(s1)); s2 = xsum32(xsum16(s2));
    if (g == 0) *(f32x2*)(ws + OFF_ST + (size_t)(tt0 * 16 + tok16) * 64 + cg * 8) = f32x2{s1, s2};
  } else if constexpr (MODE == 2) {
#pragma unroll
    for (int n = 0; n < NT; ++n) {
      u32x2 v;
      v[0] = pk2(acc[0][n][0] + bflo(res[n][0]), acc[0][n][1] + bfhi(res[n][0]));
      v[1] = pk2(acc[0][n][2] + bflo(res[n][1]), acc[0][n][3] + bfhi(res[n][1]));
      *(u32x2*)(out + (size_t)(tt0 * 16 + tok16) * 1280 + (cg * 80 + n * 16 + 4 * g) * 2) = v;
    }
  } else {
#pragma unroll
    for (int t = 0; t < TT; ++t) {
      const float nk = -1.4426950408889634f / __builtin_sqrtf(sb_[t]);
#pragma unroll
      for (int i = 0; i < 7; ++i) {
        const f32x4 cgt = ldsr<f32x4>(l0 + SLOTS * SLOT + (2 * i) * 64 + g * 16);
        const f32x4 cup = ldsr<f32x4>(l0 + SLOTS * SLOT + (2 * i + 1) * 64 + g * 16);
        float mv[4];
#pragma unroll
        for (int e = 0; e < 4; ++e) {
          const float tg = acc[t][2 * i][e] + sa[t] * cgt[e], tu = acc[t][2 * i + 1][e] + sa[t] * cup[e];
          const float ex = __builtin_amdgcn_exp2f(tg * nk);
          mv[e] = tg * tu * __builtin_amdgcn_rcpf(ex * sb_[t] + sb_[t]);
        }
        const int ft = cg == 15 ? 101 + i : 7 * cg + i;
        if (cg != 15 || i >= 4) {
          u32x2 v; v[0] = pk2(mv[0], mv[1]); v[1] = pk2(mv[2], mv[3]);
          *(u32x2*)(ws + OFF_MB + (size_t)((tt0 + t) * 54 + (ft >> 1)) * 1024 + lane * 16 + (ft & 1) * 8) = v;
        }
      }
    }
  }
}

extern "C" void kernel_launch(void* const* d_in, const int* in_sizes, int n_in, void* d_out, int out_size,
                              void* d_ws, size_t ws_size, hipStream_t stream) {
  const char* x = (const char*)d_in[0];
  char* ws = (char*)d_ws;
  k_prep<<<235, 512, 0, stream>>>((const unsigned short*)d_in[2], (const unsigned short*)d_in[4],
                                  (const unsigned short*)d_in[5], (const unsigned short*)d_in[6], ws);
  k_qkv<<<256, 512, 0, stream>>>(x, (const char*)d_in[1], ws);
  k_attn<<<256, 512, 0, stream>>>(ws);
  k_gemm<0><<<256, 512, 0, stream>>>(x, ws, (char*)d_out);
  k_gemm<1><<<256, 512, 0, stream>>>(x, ws, (char*)d_out);
  k_gemm<2><<<256, 512, 0, stream>>>(x, ws, (char*)d_out);
}
```

```cpp
#include <hip/hip_runtime.h>
#include <stdint.h>

typedef __attribute__((ext_vector_type(8))) short s16x8;
typedef __attribute__((ext_vector_type(4))) short s16x4;
typedef __attribute__((ext_vector_type(2))) __bf16 bf16x2;
typedef __attribute__((ext_vector_type(2))) float f32x2;
typedef __attribute__((ext_vector_type(4))) float f32x4;
typedef __attribute__((ext_vector_type(16))) float f32x16;
typedef __attribute__((ext_vector_type(2))) unsigned u32x2;
typedef __attribute__((ext_vector_type(4))) unsigned u32x4;
typedef __attribute__((ext_vector_type(2))) unsigned long long u64x2;
#define LDS_AS __attribute__((address_space(3)))
#define DEV __device__ __forceinline__
#define PAGE __attribute__((aligned(4096)))

constexpr int S_ = 2048, D_ = 640, F_ = 1728;
constexpr int KVP = 21504;
constexpr int QP = 10240;
constexpr size_t OFF_QB = 0;
constexpr size_t OFF_KV = OFF_QB + (size_t)32 * 8 * QP;
constexpr size_t OFF_OB = OFF_KV + (size_t)32 * 8 * KVP;
constexpr size_t OFF_HB = OFF_OB + (size_t)128 * 20 * 1024;
constexpr size_t OFF_MB = OFF_HB + (size_t)128 * 20 * 1024;
constexpr size_t OFF_WO = OFF_MB + (size_t)128 * 54 * 1024;
constexpr size_t OFF_WGU = OFF_WO + (size_t)8 * 20 * 5 * 1024;
constexpr size_t OFF_WD = OFF_WGU + (size_t)16 * 20 * 14 * 1024;
constexpr size_t OFF_CSP = OFF_WD + (size_t)8 * 54 * 5 * 1024 + 16384;
constexpr size_t OFF_CS2 = OFF_CSP + (size_t)10 * 2 * F_ * 4;
constexpr size_t OFF_ST = OFF_CS2 + (size_t)16 * 1024;
constexpr unsigned VC = 8 * 40 * 1024, VST = VC + 128;
constexpr size_t OFF_V = OFF_ST + (size_t)2048 * 8 * 8;
constexpr size_t WS_END = OFF_V + (size_t)8 * VST;

#define STAMP(k, i)
#define STAMPE(k, i)
#define STAMPH()
#define STAMPW(k)
#define TS(i)
#define MFMA32(a, b, c) __builtin_amdgcn_mfma_f32_32x32x16_bf16((a), (b), (c), 0, 0, 0)
#define MFMA32H(a, b, c) __builtin_amdgcn_mfma_f32_32x32x8bf16_1k((a), (b), (c), 0, 0, 0)
#define MFMA16(a, b, c) __builtin_amdgcn_mfma_f32_16x16x32_bf16((a), (b), (c), 0, 0, 0)

DEV void glds(const void* sbase, unsigned voff, unsigned lds) {
  unsigned keep;
  asm volatile("s_mov_b32 %0, m0\n\ts_mov_b32 m0, %3\n\ts_nop 0\n\tglobal_load_lds_dwordx4 %1, %2\n\ts_mov_b32 m0, %0"
               : "=&s"(keep) : "v"(voff), "s"(sbase), "s"(lds) : "memory");
}
DEV void setm0(unsigned lds) { asm volatile("s_mov_b32 m0, %0\n\ts_nop 0" ::"s"(lds) : "memory"); }
template <int OFF> DEV void gldso(const void* sbase, unsigned voff) {
  asm volatile("global_load_lds_dwordx4 %0, %1 offset:%2" ::"v"(voff), "s"(sbase), "i"(OFF) : "memory");
}
template <int N> DEV void waitv() { asm volatile("s_waitcnt vmcnt(%0)" ::"i"(N) : "memory"); }
DEV void bar() { asm volatile("s_barrier" ::: "memory"); }
DEV void lbar() { asm volatile("s_waitcnt lgkmcnt(0)\n\ts_barrier" ::: "memory"); }
DEV unsigned pk2(float a, float b) { f32x2 v = {a, b}; return __builtin_bit_cast(unsigned, __builtin_convertvector(v, bf16x2)); }
DEV float dot2(unsigned a, unsigned b, float c) {
  return __builtin_amdgcn_fdot2_f32_bf16(__builtin_bit_cast(bf16x2, a), __builtin_bit_cast(bf16x2, b), c, false);
}
DEV float bflo(unsigned v) { return __uint_as_float(v << 16); }
DEV float bfhi(unsigned v) { return __uint_as_float(v & 0xffff0000u); }
DEV float xsum32(float x) {
  auto r = __builtin_amdgcn_permlane32_swap(__float_as_uint(x), __float_as_uint(x), false, false);
  return __uint_as_float(r[0]) + __uint_as_float(r[1]);
}
DEV float xsum16(float x) {
  auto r = __builtin_amdgcn_permlane16_swap(__float_as_uint(x), __float_as_uint(x), false, false);
  return __uint_as_float(r[0]) + __uint_as_float(r[1]);
}
DEV float xmax32(float x) {
  auto r = __builtin_amdgcn_permlane32_swap(__float_as_uint(x), __float_as_uint(x), false, false);
  return __builtin_fmaxf(__uint_as_float(r[0]), __uint_as_float(r[1]));
}
template <class T> DEV T ldsr(unsigned a) { return *(LDS_AS T*)(size_t)a; }
template <class T> DEV void ldsw(unsigned a, T v) { *(LDS_AS T*)(size_t)a = v; }
DEV s16x4 ldstr(unsigned a) { return __builtin_amdgcn_ds_read_tr16_b64_v4i16((LDS_AS s16x4*)(size_t)a); }
#define PIN() do { asm volatile("" ::: "memory"); __builtin_amdgcn_sched_barrier(0); } while (0)
constexpr int cmin(int a, int b) { return a < b ? a : b; }
constexpr int cmax(int a, int b) { return a > b ? a : b; }
constexpr int ring_issued(int b, int ns, int slots, int pre) {
  return b == 0 ? cmin(pre, ns) : cmin(ns, cmin(pre + 2 * b, cmax(slots, b - 1 + slots - 1)));
}
typedef __amdgpu_buffer_rsrc_t rsrc_t;
DEV rsrc_t mkrs(const void* p, unsigned bytes) { return __builtin_amdgcn_make_buffer_rsrc((void*)p, (short)0, (int)bytes, 0x00020000); }
#define ZROW 21
#define PREPAUX 16
#define OUTAUX 0
#define STAUX 0
DEV u32x4 bld16(rsrc_t r, unsigned v, unsigned s) { return __builtin_amdgcn_raw_buffer_load_b128(r, v, s, 0); }
DEV u32x2 bld8(rsrc_t r, unsigned v, unsigned s) { return __builtin_amdgcn_raw_buffer_load_b64(r, v, s, 0); }
template <int AUX = STAUX> DEV void bst16(u32x4 d, rsrc_t r, unsigned v, unsigned s) {
  __builtin_amdgcn_raw_buffer_store_b128(d, r, v, s, AUX);
  asm volatile("s_nop 1" ::"v"(d) : "memory");
}
template <int AUX = STAUX> DEV void bst8(u32x2 d, rsrc_t r, unsigned v, unsigned s) { __builtin_amdgcn_raw_buffer_store_b64(d, r, v, s, AUX); }
#define K2A 16
#define K3A 16
#define K4A 16
#define K4B 0
#define K4C 0
DEV void mfma16_acc(f32x4& c, s16x8 a, s16x8 b) { asm volatile("v_mfma_f32_16x16x32_bf16 %0, %1, %2, %0" : "+v"(c) : "v"(a), "v"(b)); }
DEV void mfma16_new(f32x4& c, s16x8 a, s16x8 b) { asm volatile("v_mfma_f32_16x16x32_bf16 %0, %1, %2, 0" : "=&v"(c) : "v"(a), "v"(b)); }
DEV void mfma32_acc(f32x16& c, s16x8 a, s16x8 b) { asm volatile("v_mfma_f32_32x32x16_bf16 %0, %1, %2, %0" : "+v"(c) : "v"(a), "v"(b)); }
DEV void mfma32h_new(f32x16& d, s16x4 a, s16x4 b, const f32x16& c) { asm volatile("v_mfma_f32_32x32x8_bf16 %0, %1, %2, %3" : "=&v"(d) : "v"(a), "v"(b), "v"(c)); }
DEV void mfma4_acc(f32x4& c, s16x4 a, s16x4 b) { asm volatile("v_mfma_f32_4x4x4_16b_bf16 %0, %1, %2, %0" : "+v"(c) : "v"(a), "v"(b)); }
DEV s16x4 lo4(s16x8 v) { return __builtin_shufflevector(v, v, 0, 1, 2, 3); }
DEV s16x4 hi4(s16x8 v) { return __builtin_shufflevector(v, v, 4, 5, 6, 7); }
#define PAGE_MACROS() asm volatile( \
    ".macro pad k, n\n.L\\k\\()_pad\\n:\n s_cbranch_execz .L\\k\\()_ret\n.endm\n" \
    ".macro newpage k\n s_branch 9f\n .fill (4096 - ((. - \\k) & 4095)) / 4, 4, 0xbf800000\n9:\n .set .L\\k\\()_pn, (. - \\k) >> 12\n.endm\n" \
    ".macro padhere k\n .if ((. - \\k) >> 12) > .L\\k\\()_pn\n .set .L\\k\\()_pn, (. - \\k) >> 12\n .set .L\\k\\()_pc, .L\\k\\()_pc + 1\n" \
    " .if .L\\k\\()_pc == 1\n pad \\k, 3\n .elseif .L\\k\\()_pc == 2\n pad \\k, 4\n .else\n pad \\k, 5\n .endif\n .endif\n.endm\n" \
    ".macro visit k, c, n\n.L\\k\\()_ret:\n s_add_u32 \\c, \\c, 4\n .irp i, 0, 1, 2, 3, 4, 5\n .if (\\n >> \\i) & 1\n" \
    " s_cmp_eq_u32 \\c, \\i + 4\n s_cbranch_scc1 .L\\k\\()_pad\\i\n .endif\n .endr\n.endm\n" \
    ".irp k, k_qkv, k_attn, k_oproj, k_gu, k_down\n .set .L\\k\\()_pn, 0\n .set .L\\k\\()_pc, 0\n.endr")
#define NEWPAGE(K) asm volatile("newpage " K ::: "memory")
#define PAD(K, N) do { asm volatile("pad " K ", " #N ::: "memory"); __builtin_amdgcn_sched_barrier(0); } while (0)
#define PADHERE(K) asm volatile("padhere " K ::: "memory")
#define VISIT(K, W, MASK) do { unsigned long long e_; int c_ = (W); \
  asm volatile("s_mov_b64 %0, exec\n\ts_mov_b64 exec, 0\n\tvisit " K ", %1, " #MASK "\n\ts_mov_b64 exec, %0" : "=&s"(e_), "+s"(c_) :: "scc", "memory"); } while (0)
template <int I> struct IC { static constexpr int v = I; };
template <int B, int E, class Fn> DEV void sfor(Fn&& f) {
  if constexpr (B < E) { f(IC<B>{}); sfor<B + 1, E>(f); }
}

DEV void prep_job(int J, const unsigned short* wo, const unsigned short* wg, const unsigned short* wu,
                  const unsigned short* wd, char* ws, int lane) {
  const int g = lane >> 4, m = lane & 15;
  const unsigned short* src; unsigned ld2, vo, so, es, e2, hs, bs, dv[4], dx[4], dk, cs = 0;
  if (J >= 100 && J < 640) {
    const int U = J - 100, gu = U >= 270, V = U - 270 * gu, p = V / 27, G = V % 27;
    src = gu ? wu : wg; ld2 = F_ * 2;
    vo = 4 * g * ld2 + m * 8; so = 64 * p * ld2 + G * 128; es = ld2; e2 = 2 * ld2; hs = 16 * ld2; bs = 32 * ld2;
#pragma unroll
    for (int j = 0; j < 4; ++j) {
      const int t = 4 * G + j, c = t / 7, x = t % 7 - (c & 1), ix = (0x6B49720 >> (4 * (x < 0 ? 6 : x))) & 15;
      dv[j] = (unsigned)OFF_WGU + (c * 20 + 2 * p) * 14336 + ix * 1024 + gu * 512;
      dx[j] = (m >> 3) * (ix == 6 ? 7168 : 1024) + (g ^ gu) * 128 + (m & 7) * 16;
    }
    dk = 14336; cs = (unsigned)OFF_CSP + ((p * 2 + gu) * F_ + 64 * G) * 4;
  } else {
    const bool isd = J >= 640;
    const int U = isd ? J - 640 : J, p = U / 10, G = U % 10;
    src = isd ? wd : wo; ld2 = D_ * 2;
    so = (isd ? 64 * p * ld2 : 0) + G * 128;
    if (isd) { vo = 8 * g * ld2 + m * 8; es = 4 * ld2; e2 = 32 * ld2; hs = ld2; bs = 2 * ld2; }
    else { vo = 4 * g * ld2 + m * 8; es = ld2; e2 = 2 * ld2; hs = 16 * ld2; bs = 32 * ld2; }
#pragma unroll
    for (int j = 0; j < 4; ++j) dv[j] = (isd ? (unsigned)OFF_WD : (unsigned)OFF_WO) + 2 * p * 5120 + j * 16;
    dk = 5120 | (isd ? 54 : 20) | (G << 16);
  }
  const rsrc_t rs = mkrs(src, 0x7fffffff), rw = mkrs(ws, WS_END);
  u32x2 x[2][2][4];
#pragma unroll
  for (int b = 0; b < 2; ++b)
#pragma unroll
    for (int hf = 0; hf < 2; ++hf) {
      const int sl = 8 * (J / 10) + 4 * b + g, j5 = (sl * 205) >> 10;
      const unsigned vb = J < 100 ? (10 * j5 + (((hf ? 0x98732 : 0x46510) >> (4 * (sl - 5 * j5))) & 15)) * (8 * D_) + m * 8 : vo + b * bs + hf * hs;
#pragma unroll
      for (int e = 0; e < 4; ++e) x[b][hf][e] = bld8(rs, vb, so + (e & 1) * es + (e >> 1) * e2);
    }
  if (!cs) {
    const int G = dk >> 16, nt = 4 * G + (m >> 2);
    int cg = (nt * 205) >> 10, n = nt - 5 * cg, i = m & 3;
    if (J >= 640) {
      if (G < 8) { cg = G; n = 2 * (m >> 3) + (m & 1); i = (m >> 1) & 3; }
      else { cg = nt - 32; n = 4; }
    }
#pragma unroll
    for (int j = 0; j < 4; ++j) dx[j] = (cg * (dk & 63) * 5 + n) * 1024 + (g * 16 + 4 * i) * 16;
    dk = 5120;
  }
  const s16x8 ones = {0x3f80, 0x3f80, 0x3f80, 0x3f80, 0x3f80, 0x3f80, 0x3f80, 0x3f80};
  f32x4 acc[4];
#pragma unroll
  for (int j = 0; j < 4; ++j) acc[j] = f32x4{0.f, 0.f, 0.f, 0.f};
  u32x4 v[2][4];
#pragma unroll
  for (int b = 0; b < 2; ++b)
#pragma unroll
    for (int j = 0; j < 4; ++j) {
#pragma unroll
      for (int q = 0; q < 4; ++q)
        v[b][j][q] = __builtin_amdgcn_perm(x[b][q >> 1][2 * (q & 1) + 1][j >> 1], x[b][q >> 1][2 * (q & 1)][j >> 1], (j & 1) ? 0x07060302u : 0x05040100u);
      __builtin_amdgcn_raw_buffer_store_b128(v[b][j], rw, dx[j], dv[j] + b * dk, PREPAUX);
    }
  asm volatile("s_nop 1" ::"v"(v[0][0]), "v"(v[0][1]), "v"(v[0][2]), "v"(v[0][3]), "v"(v[1][0]), "v"(v[1][1]), "v"(v[1][2]), "v"(v[1][3]) : "memory");
  if (cs) {
#pragma unroll
    for (int b = 0; b < 2; ++b)
#pragma unroll
      for (int j = 0; j < 4; ++j) acc[j] = MFMA16(ones, __builtin_bit_cast(s16x8, v[b][j]), acc[j]);
    if (lane < 16) {
      const u32x4 c = {__float_as_uint(acc[0][0]), __float_as_uint(acc[1][0]), __float_as_uint(acc[2][0]), __float_as_uint(acc[3][0])};
      __builtin_amdgcn_raw_buffer_store_b128(c, rw, lane * 16, cs, PREPAUX);
      asm volatile("s_nop 1" ::"v"(c) : "memory");
    }
  }
}

#define K1S0 2
#define K1PRE 2
#define K1RING 3
#define K1RAMP 2
#define QK_LOOP \
  "ds_read_b128 v[108:111], %[x00] offset:0\nds_read_b64_tr_b16 v[140:141], %[w0] offset:0\nds_read_b64_tr_b16 v[142:143], %[w0] offset:1024\n" \
  "ds_read_b64_tr_b16 v[144:145], %[w1] offset:0\nds_read_b64_tr_b16 v[146:147], %[w1] offset:1024\nds_read_b128 v[112:115], %[x00] offset:2048\n" \
  "ds_read_b64_tr_b16 v[148:149], %[w2] offset:0\nds_read_b64_tr_b16 v[150:151], %[w2] offset:1024\nds_read_b128 v[116:119], %[x01] offset:0\n" \
  "ds_read_b64_tr_b16 v[152:153], %[w3] offset:0\nds_read_b64_tr_b16 v[154:155], %[w3] offset:1024\nds_read_b128 v[120:123], %[x01] offset:2048\n" \
  "s_mov_b32 %[c], 0\ns_waitcnt lgkmcnt(9)\nv_mfma_f32_16x16x32_bf16 v[16:19], v[140:143], v[108:111], 0\nds_read_b128 v[124:127], %[x10] offset:0\n" \
  "s_waitcnt lgkmcnt(8)\nv_mfma_f32_16x16x32_bf16 v[20:23], v[144:147], v[108:111], 0\nds_read_b64_tr_b16 v[156:157], %[w0] offset:8192\n" \
  "v_mfma_f32_16x16x32_bf16 v[80:83], v[104:107], v[108:111], 0\nds_read_b64_tr_b16 v[158:159], %[w0] offset:9216\ns_waitcnt lgkmcnt(9)\n" \
  "v_mfma_f32_16x16x32_bf16 v[32:35], v[140:143], v[112:115], 0\nds_read_b64_tr_b16 v[160:161], %[w1] offset:8192\n" \
  "v_mfma_f32_16x16x32_bf16 v[36:39], v[144:147], v[112:115], 0\nds_read_b64_tr_b16 v[162:163], %[w1] offset:9216\n" \
  "v_mfma_f32_16x16x32_bf16 v[88:91], v[108:111], v[108:111], 0\nds_read_b128 v[128:131], %[x10] offset:2048\ns_waitcnt lgkmcnt(10)\n" \
  "v_mfma_f32_16x16x32_bf16 v[24:27], v[148:151], v[108:111], 0\nds_read_b64_tr_b16 v[164:165], %[w2] offset:8192\n" \
  "v_mfma_f32_16x16x32_bf16 v[40:43], v[148:151], v[112:115], 0\nds_read_b64_tr_b16 v[166:167], %[w2] offset:9216\n" \
  "v_mfma_f32_16x16x32_bf16 v[96:99], v[104:107], v[140:143], 0\nds_read_b128 v[132:135], %[x11] offset:0\ns_waitcnt lgkmcnt(12)\n" \
  "v_mfma_f32_16x16x32_bf16 v[48:51], v[140:143], v[116:119], 0\nds_read_b64_tr_b16 v[168:169], %[w3] offset:8192\n" \
  "v_mfma_f32_16x16x32_bf16 v[52:55], v[144:147], v[116:119], 0\nds_read_b64_tr_b16 v[170:171], %[w3] offset:9216\n" \
  "v_mfma_f32_16x16x32_bf16 v[100:103], v[104:107], v[144:147], 0\nds_read_b128 v[136:139], %[x11] offset:2048\n" \
  "v_mfma_f32_16x16x32_bf16 v[56:59], v[148:151], v[116:119], 0\nv_add_u32 %[x00], 0x8000, %[x00]\ns_waitcnt lgkmcnt(13)\n" \
  "v_mfma_f32_16x16x32_bf16 v[28:31], v[152:155], v[108:111], 0\nv_add_u32 %[w0], 0x8000, %[w0]\n" \
  "v_mfma_f32_16x16x32_bf16 v[84:87], v[104:107], v[112:115], 0\nv_add_u32 %[w1], 0x8000, %[w1]\n" \
  "v_mfma_f32_16x16x32_bf16 v[44:47], v[152:155], v[112:115], 0\nv_add_u32 %[x01], 0x8000, %[x01]\n" \
  "v_mfma_f32_16x16x32_bf16 v[60:63], v[152:155], v[116:119], 0\nv_add_u32 %[w2], 0x8000, %[w2]\n" \
  "v_mfma_f32_16x16x32_bf16 v[92:95], v[112:115], v[112:115], 0\nv_add_u32 %[x10], 0x8000, %[x10]\ns_waitcnt lgkmcnt(12)\n" \
  "v_mfma_f32_16x16x32_bf16 v[64:67], v[140:143], v[120:123], 0\nv_add_u32 %[w3], 0x8000, %[w3]\n" \
  "v_mfma_f32_16x16x32_bf16 v[68:71], v[144:147], v[120:123], 0\nv_add_u32 %[x11], 0x8000, %[x11]\n" \
  "v_mfma_f32_16x16x32_bf16 v[72:75], v[148:151], v[120:123], 0\ns_waitcnt lgkmcnt(0)\nv_mfma_f32_16x16x32_bf16 v[76:79], v[152:155], v[120:123], 0\n1:\n" \
  "s_barrier\nv_mfma_f32_16x16x32_bf16 v[16:19], v[156:159], v[124:127], v[16:19]\nds_read_b128 v[108:111], %[x00] offset:0\n" \
  "v_mfma_f32_16x16x32_bf16 v[20:23], v[160:163], v[124:127], v[20:23]\nds_read_b64_tr_b16 v[140:141], %[w0] offset:0\n" \
  "v_mfma_f32_16x16x32_bf16 v[80:83], v[104:107], v[124:127], v[80:83]\nds_read_b64_tr_b16 v[142:143], %[w0] offset:1024\n" \
  "v_mfma_f32_16x16x32_bf16 v[32:35], v[156:159], v[128:131], v[32:35]\nds_read_b64_tr_b16 v[144:145], %[w1] offset:0\n" \
  "v_mfma_f32_16x16x32_bf16 v[36:39], v[160:163], v[128:131], v[36:39]\nds_read_b64_tr_b16 v[146:147], %[w1] offset:1024\n" \
  "v_mfma_f32_16x16x32_bf16 v[88:91], v[124:127], v[124:127], v[88:91]\nds_read_b128 v[112:115], %[x00] offset:2048\n" \
  "v_mfma_f32_16x16x32_bf16 v[24:27], v[164:167], v[124:127], v[24:27]\nds_read_b64_tr_b16 v[148:149], %[w2] offset:0\n" \
  "v_mfma_f32_16x16x32_bf16 v[40:43], v[164:167], v[128:131], v[40:43]\nds_read_b64_tr_b16 v[150:151], %[w2] offset:1024\n" \
  "v_mfma_f32_16x16x32_bf16 v[96:99], v[104:107], v[156:159], v[96:99]\nds_read_b128 v[116:119], %[x01] offset:0\n" \
  "v_mfma_f32_16x16x32_bf16 v[48:51], v[156:159], v[132:135], v[48:51]\nds_read_b64_tr_b16 v[152:153], %[w3] offset:0\n" \
  "v_mfma_f32_16x16x32_bf16 v[52:55], v[160:163], v[132:135], v[52:55]\nds_read_b64_tr_b16 v[154:155], %[w3] offset:1024\n" \
  "v_mfma_f32_16x16x32_bf16 v[100:103], v[104:107], v[160:163], v[100:103]\nds_read_b128 v[120:123], %[x01] offset:2048\n" \
  "v_mfma_f32_16x16x32_bf16 v[56:59], v[164:167], v[132:135], v[56:59]\nv_mfma_f32_16x16x32_bf16 v[28:31], v[168:171], v[124:127], v[28:31]\n" \
  "v_mfma_f32_16x16x32_bf16 v[84:87], v[104:107], v[128:131], v[84:87]\nv_mfma_f32_16x16x32_bf16 v[44:47], v[168:171], v[128:131], v[44:47]\n" \
  "v_mfma_f32_16x16x32_bf16 v[60:63], v[168:171], v[132:135], v[60:63]\nv_mfma_f32_16x16x32_bf16 v[92:95], v[128:131], v[128:131], v[92:95]\n" \
  "s_add_u32 %[c], %[c], 1\nv_mfma_f32_16x16x32_bf16 v[64:67], v[156:159], v[136:139], v[64:67]\ns_cmp_eq_u32 %[c], 4\n" \
  "v_mfma_f32_16x16x32_bf16 v[68:71], v[160:163], v[136:139], v[68:71]\ns_cselect_b32 %[dl], %[wrap], 0x8000\n" \
  "v_mfma_f32_16x16x32_bf16 v[72:75], v[164:167], v[136:139], v[72:75]\ns_waitcnt lgkmcnt(0)\n" \
  "v_mfma_f32_16x16x32_bf16 v[76:79], v[168:171], v[136:139], v[76:79]\nv_mfma_f32_16x16x32_bf16 v[16:19], v[140:143], v[108:111], v[16:19]\n" \
  "ds_read_b128 v[124:127], %[x10] offset:0\nv_mfma_f32_16x16x32_bf16 v[20:23], v[144:147], v[108:111], v[20:23]\n" \
  "ds_read_b64_tr_b16 v[156:157], %[w0] offset:8192\nv_mfma_f32_16x16x32_bf16 v[80:83], v[104:107], v[108:111], v[80:83]\n" \
  "ds_read_b64_tr_b16 v[158:159], %[w0] offset:9216\nv_mfma_f32_16x16x32_bf16 v[32:35], v[140:143], v[112:115], v[32:35]\n" \
  "ds_read_b64_tr_b16 v[160:161], %[w1] offset:8192\nv_mfma_f32_16x16x32_bf16 v[36:39], v[144:147], v[112:115], v[36:39]\n" \
  "ds_read_b64_tr_b16 v[162:163], %[w1] offset:9216\nv_mfma_f32_16x16x32_bf16 v[88:91], v[108:111], v[108:111], v[88:91]\n" \
  "ds_read_b128 v[128:131], %[x10] offset:2048\nv_mfma_f32_16x16x32_bf16 v[24:27], v[148:151], v[108:111], v[24:27]\n" \
  "ds_read_b64_tr_b16 v[164:165], %[w2] offset:8192\nv_mfma_f32_16x16x32_bf16 v[40:43], v[148:151], v[112:115], v[40:43]\n" \
  "ds_read_b64_tr_b16 v[166:167], %[w2] offset:9216\nv_mfma_f32_16x16x32_bf16 v[96:99], v[104:107], v[140:143], v[96:99]\n" \
  "ds_read_b128 v[132:135], %[x11] offset:0\nv_mfma_f32_16x16x32_bf16 v[48:51], v[140:143], v[116:119], v[48:51]\n" \
  "ds_read_b64_tr_b16 v[168:169], %[w3] offset:8192\nv_mfma_f32_16x16x32_bf16 v[52:55], v[144:147], v[116:119], v[52:55]\n" \
  "ds_read_b64_tr_b16 v[170:171], %[w3] offset:9216\nv_mfma_f32_16x16x32_bf16 v[100:103], v[104:107], v[144:147], v[100:103]\n" \
  "ds_read_b128 v[136:139], %[x11] offset:2048\nv_mfma_f32_16x16x32_bf16 v[56:59], v[148:151], v[116:119], v[56:59]\nv_add_u32 %[x00], %[dl], %[x00]\n" \
  "v_mfma_f32_16x16x32_bf16 v[28:31], v[152:155], v[108:111], v[28:31]\nv_add_u32 %[w0], %[dl], %[w0]\n" \
  "v_mfma_f32_16x16x32_bf16 v[84:87], v[104:107], v[112:115], v[84:87]\nv_add_u32 %[w1], %[dl], %[w1]\n" \
  "v_mfma_f32_16x16x32_bf16 v[44:47], v[152:155], v[112:115], v[44:47]\nv_add_u32 %[x01], %[dl], %[x01]\n" \
  "v_mfma_f32_16x16x32_bf16 v[60:63], v[152:155], v[116:119], v[60:63]\nv_add_u32 %[w2], %[dl], %[w2]\n" \
  "v_mfma_f32_16x16x32_bf16 v[92:95], v[112:115], v[112:115], v[92:95]\nv_add_u32 %[x10], %[dl], %[x10]\n" \
  "v_mfma_f32_16x16x32_bf16 v[64:67], v[140:143], v[120:123], v[64:67]\nv_add_u32 %[w3], %[dl], %[w3]\n" \
  "v_mfma_f32_16x16x32_bf16 v[68:71], v[144:147], v[120:123], v[68:71]\nv_add_u32 %[x11], %[dl], %[x11]\ns_cmp_lg_u32 %[c], 9\n" \
  "v_mfma_f32_16x16x32_bf16 v[72:75], v[148:151], v[120:123], v[72:75]\ns_waitcnt lgkmcnt(0)\n" \
  "v_mfma_f32_16x16x32_bf16 v[76:79], v[152:155], v[120:123], v[76:79]\ns_cbranch_scc1 1b\n" \
  "v_mfma_f32_16x16x32_bf16 v[88:91], v[124:127], v[124:127], v[88:91]\nv_mfma_f32_16x16x32_bf16 v[92:95], v[128:131], v[128:131], v[92:95]\n" \
  "v_mfma_f32_16x16x32_bf16 v[80:83], v[104:107], v[124:127], v[80:83]\nv_mfma_f32_16x16x32_bf16 v[84:87], v[104:107], v[128:131], v[84:87]\n" \
  "v_cndmask_b32_e64 v108, v88, v89, %[mc1]\nv_cndmask_b32_e64 v109, v90, v91, %[mc1]\n" \
  "v_mfma_f32_16x16x32_bf16 v[96:99], v[104:107], v[156:159], v[96:99]\nv_cndmask_b32_e64 v118, v92, v93, %[mc1]\n" \
  "v_cndmask_b32_e64 v119, v94, v95, %[mc1]\nv_mfma_f32_16x16x32_bf16 v[100:103], v[104:107], v[160:163], v[100:103]\n" \
  "v_cndmask_b32_e64 v110, v108, v109, %[mc2]\nv_cndmask_b32_e64 v111, v118, v119, %[mc2]\n" \
  "v_mfma_f32_16x16x32_bf16 v[16:19], v[156:159], v[124:127], v[16:19]\nv_mul_f32 v112, 0x3acccccd, v80\nv_mul_f32 v113, 0x3acccccd, v84\n" \
  "v_mfma_f32_16x16x32_bf16 v[20:23], v[160:163], v[124:127], v[20:23]\n" \
  "v_pk_fma_f32 v[110:111], v[110:111], %[kc], %[kc] op_sel:[0,0,1] op_sel_hi:[1,0,1]\n" \
  "v_pk_fma_f32 v[110:111], v[112:113], v[112:113], v[110:111] neg_lo:[1,0,0] neg_hi:[1,0,0]\n" \
  "v_mfma_f32_16x16x32_bf16 v[24:27], v[164:167], v[124:127], v[24:27]\nv_max_f32 v110, 0x3727c5ac, v110\nv_max_f32 v111, 0x3727c5ac, v111\n" \
  "v_mfma_f32_16x16x32_bf16 v[28:31], v[168:171], v[124:127], v[28:31]\nv_rsq_f32 v110, v110\nds_write2_b32 %[cw], v96, v100 offset0:0 offset1:16\n" \
  "v_mfma_f32_16x16x32_bf16 v[32:35], v[156:159], v[128:131], v[32:35]\nv_rsq_f32 v111, v111\n" \
  "v_mfma_f32_16x16x32_bf16 v[36:39], v[160:163], v[128:131], v[36:39]\nv_pk_mul_f32 v[114:115], v[110:111], %[qs] op_sel_hi:[1,0]\n" \
  "v_pk_mul_f32 v[116:117], v[112:113], v[114:115] neg_lo:[1,0] neg_hi:[1,0]\nv_mfma_f32_16x16x32_bf16 v[40:43], v[164:167], v[128:131], v[40:43]\n" \
  "ds_write2_b32 %[sw], v114, v116 offset0:0 offset1:1\nds_write2_b32 %[sw], v115, v117 offset0:32 offset1:33\n" \
  "v_mfma_f32_16x16x32_bf16 v[44:47], v[168:171], v[128:131], v[44:47]\nv_mfma_f32_16x16x32_bf16 v[48:51], v[156:159], v[132:135], v[48:51]\n" \
  "v_mfma_f32_16x16x32_bf16 v[52:55], v[160:163], v[132:135], v[52:55]\ns_waitcnt lgkmcnt(0)\ns_barrier\n" \
  "v_mfma_f32_16x16x32_bf16 v[56:59], v[164:167], v[132:135], v[56:59]\nds_read2_b64 v[108:111], %[ra0] offset0:0 offset1:16\n" \
  "ds_read_b128 v[140:143], %[rc0] offset:0\nv_mfma_f32_16x16x32_bf16 v[60:63], v[168:171], v[132:135], v[60:63]\n" \
  "ds_read_b128 v[144:147], %[rc0] offset:64\nds_read_b128 v[148:151], %[rc1] offset:0\n" \
  "v_mfma_f32_16x16x32_bf16 v[64:67], v[156:159], v[136:139], v[64:67]\nds_read_b128 v[152:155], %[rc1] offset:64\n" \
  "ds_read2_b64 v[112:115], %[ra1] offset0:0 offset1:16\nv_mfma_f32_16x16x32_bf16 v[68:71], v[160:163], v[136:139], v[68:71]\n" \
  "v_mfma_f32_16x16x32_bf16 v[72:75], v[164:167], v[136:139], v[72:75]\nv_mfma_f32_16x16x32_bf16 v[76:79], v[168:171], v[136:139], v[76:79]\n" \
  "s_waitcnt lgkmcnt(0)\n"
#define V_LOOP \
  "ds_read_b128 v[108:111], %[x00] offset:0\nds_read_b64_tr_b16 v[140:141], %[w0] offset:0\nds_read_b64_tr_b16 v[142:143], %[w0] offset:1024\n" \
  "ds_read_b64_tr_b16 v[144:145], %[w1] offset:0\nds_read_b64_tr_b16 v[146:147], %[w1] offset:1024\nds_read_b128 v[112:115], %[x00] offset:1024\n" \
  "ds_read_b64_tr_b16 v[148:149], %[w2] offset:0\nds_read_b64_tr_b16 v[150:151], %[w2] offset:1024\nds_read_b128 v[116:119], %[x01] offset:0\n" \
  "ds_read_b64_tr_b16 v[152:153], %[w3] offset:0\nds_read_b64_tr_b16 v[154:155], %[w3] offset:1024\nds_read_b128 v[120:123], %[x01] offset:1024\n" \
  "s_mov_b32 %[c], 0\ns_waitcnt lgkmcnt(9)\nv_mfma_f32_16x16x32_bf16 v[16:19], v[108:111], v[140:143], 0\nds_read_b128 v[124:127], %[x10] offset:0\n" \
  "s_waitcnt lgkmcnt(8)\nv_mfma_f32_16x16x32_bf16 v[20:23], v[108:111], v[144:147], 0\nds_read_b64_tr_b16 v[156:157], %[w0] offset:8192\n" \
  "v_mfma_f32_16x16x32_bf16 v[80:83], v[104:107], v[108:111], 0\nds_read_b64_tr_b16 v[158:159], %[w0] offset:9216\ns_waitcnt lgkmcnt(9)\n" \
  "v_mfma_f32_16x16x32_bf16 v[32:35], v[112:115], v[140:143], 0\nds_read_b64_tr_b16 v[160:161], %[w1] offset:8192\n" \
  "v_mfma_f32_16x16x32_bf16 v[36:39], v[112:115], v[144:147], 0\nds_read_b64_tr_b16 v[162:163], %[w1] offset:9216\n" \
  "v_mfma_f32_16x16x32_bf16 v[88:91], v[108:111], v[108:111], 0\nds_read_b128 v[128:131], %[x10] offset:1024\ns_waitcnt lgkmcnt(10)\n" \
  "v_mfma_f32_16x16x32_bf16 v[24:27], v[108:111], v[148:151], 0\nds_read_b64_tr_b16 v[164:165], %[w2] offset:8192\n" \
  "v_mfma_f32_16x16x32_bf16 v[40:43], v[112:115], v[148:151], 0\nds_read_b64_tr_b16 v[166:167], %[w2] offset:9216\n" \
  "v_mfma_f32_16x16x32_bf16 v[96:99], v[104:107], v[140:143], 0\nds_read_b128 v[132:135], %[x11] offset:0\ns_waitcnt lgkmcnt(12)\n" \
  "v_mfma_f32_16x16x32_bf16 v[48:51], v[116:119], v[140:143], 0\nds_read_b64_tr_b16 v[168:169], %[w3] offset:8192\n" \
  "v_mfma_f32_16x16x32_bf16 v[52:55], v[116:119], v[144:147], 0\nds_read_b64_tr_b16 v[170:171], %[w3] offset:9216\n" \
  "v_mfma_f32_16x16x32_bf16 v[100:103], v[104:107], v[144:147], 0\nds_read_b128 v[136:139], %[x11] offset:1024\n" \
  "v_mfma_f32_16x16x32_bf16 v[56:59], v[116:119], v[148:151], 0\nv_add_u32 %[x00], 0x8000, %[x00]\ns_waitcnt lgkmcnt(13)\n" \
  "v_mfma_f32_16x16x32_bf16 v[28:31], v[108:111], v[152:155], 0\nv_add_u32 %[w0], 0x8000, %[w0]\n" \
  "v_mfma_f32_16x16x32_bf16 v[84:87], v[104:107], v[112:115], 0\nv_add_u32 %[w1], 0x8000, %[w1]\n" \
  "v_mfma_f32_16x16x32_bf16 v[44:47], v[112:115], v[152:155], 0\nv_add_u32 %[x01], 0x8000, %[x01]\n" \
  "v_mfma_f32_16x16x32_bf16 v[60:63], v[116:119], v[152:155], 0\nv_add_u32 %[w2], 0x8000, %[w2]\n" \
  "v_mfma_f32_16x16x32_bf16 v[92:95], v[112:115], v[112:115], 0\nv_add_u32 %[x10], 0x8000, %[x10]\ns_waitcnt lgkmcnt(12)\n" \
  "v_mfma_f32_16x16x32_bf16 v[64:67], v[120:123], v[140:143], 0\nv_add_u32 %[w3], 0x8000, %[w3]\n" \
  "v_mfma_f32_16x16x32_bf16 v[68:71], v[120:123], v[144:147], 0\nv_add_u32 %[x11], 0x8000, %[x11]\n" \
  "v_mfma_f32_16x16x32_bf16 v[72:75], v[120:123], v[148:151], 0\ns_waitcnt lgkmcnt(0)\nv_mfma_f32_16x16x32_bf16 v[76:79], v[120:123], v[152:155], 0\n1:\n" \
  "s_barrier\nv_mfma_f32_16x16x32_bf16 v[16:19], v[124:127], v[156:159], v[16:19]\nds_read_b128 v[108:111], %[x00] offset:0\n" \
  "v_mfma_f32_16x16x32_bf16 v[20:23], v[124:127], v[160:163], v[20:23]\nds_read_b64_tr_b16 v[140:141], %[w0] offset:0\n" \
  "v_mfma_f32_16x16x32_bf16 v[80:83], v[104:107], v[124:127], v[80:83]\nds_read_b64_tr_b16 v[142:143], %[w0] offset:1024\n" \
  "v_mfma_f32_16x16x32_bf16 v[32:35], v[128:131], v[156:159], v[32:35]\nds_read_b64_tr_b16 v[144:145], %[w1] offset:0\n" \
  "v_mfma_f32_16x16x32_bf16 v[36:39], v[128:131], v[160:163], v[36:39]\nds_read_b64_tr_b16 v[146:147], %[w1] offset:1024\n" \
  "v_mfma_f32_16x16x32_bf16 v[88:91], v[124:127], v[124:127], v[88:91]\nds_read_b128 v[112:115], %[x00] offset:1024\n" \
  "v_mfma_f32_16x16x32_bf16 v[24:27], v[124:127], v[164:167], v[24:27]\nds_read_b64_tr_b16 v[148:149], %[w2] offset:0\n" \
  "v_mfma_f32_16x16x32_bf16 v[40:43], v[128:131], v[164:167], v[40:43]\nds_read_b64_tr_b16 v[150:151], %[w2] offset:1024\n" \
  "v_mfma_f32_16x16x32_bf16 v[96:99], v[104:107], v[156:159], v[96:99]\nds_read_b128 v[116:119], %[x01] offset:0\n" \
  "v_mfma_f32_16x16x32_bf16 v[48:51], v[132:135], v[156:159], v[48:51]\nds_read_b64_tr_b16 v[152:153], %[w3] offset:0\n" \
  "v_mfma_f32_16x16x32_bf16 v[52:55], v[132:135], v[160:163], v[52:55]\nds_read_b64_tr_b16 v[154:155], %[w3] offset:1024\n" \
  "v_mfma_f32_16x16x32_bf16 v[100:103], v[104:107], v[160:163], v[100:103]\nds_read_b128 v[120:123], %[x01] offset:1024\n" \
  "v_mfma_f32_16x16x32_bf16 v[56:59], v[132:135], v[164:167], v[56:59]\nv_mfma_f32_16x16x32_bf16 v[28:31], v[124:127], v[168:171], v[28:31]\n" \
  "v_mfma_f32_16x16x32_bf16 v[84:87], v[104:107], v[128:131], v[84:87]\nv_mfma_f32_16x16x32_bf16 v[44:47], v[128:131], v[168:171], v[44:47]\n" \
  "v_mfma_f32_16x16x32_bf16 v[60:63], v[132:135], v[168:171], v[60:63]\nv_mfma_f32_16x16x32_bf16 v[92:95], v[128:131], v[128:131], v[92:95]\n" \
  "s_add_u32 %[c], %[c], 1\nv_mfma_f32_16x16x32_bf16 v[64:67], v[136:139], v[156:159], v[64:67]\ns_cmp_eq_u32 %[c], 4\n" \
  "v_mfma_f32_16x16x32_bf16 v[68:71], v[136:139], v[160:163], v[68:71]\ns_cselect_b32 %[dl], %[wrap], 0x8000\n" \
  "v_mfma_f32_16x16x32_bf16 v[72:75], v[136:139], v[164:167], v[72:75]\ns_waitcnt lgkmcnt(0)\n" \
  "v_mfma_f32_16x16x32_bf16 v[76:79], v[136:139], v[168:171], v[76:79]\nv_mfma_f32_16x16x32_bf16 v[16:19], v[108:111], v[140:143], v[16:19]\n" \
  "ds_read_b128 v[124:127], %[x10] offset:0\nv_mfma_f32_16x16x32_bf16 v[20:23], v[108:111], v[144:147], v[20:23]\n" \
  "ds_read_b64_tr_b16 v[156:157], %[w0] offset:8192\nv_mfma_f32_16x16x32_bf16 v[80:83], v[104:107], v[108:111], v[80:83]\n" \
  "ds_read_b64_tr_b16 v[158:159], %[w0] offset:9216\nv_mfma_f32_16x16x32_bf16 v[32:35], v[112:115], v[140:143], v[32:35]\n" \
  "ds_read_b64_tr_b16 v[160:161], %[w1] offset:8192\nv_mfma_f32_16x16x32_bf16 v[36:39], v[112:115], v[144:147], v[36:39]\n" \
  "ds_read_b64_tr_b16 v[162:163], %[w1] offset:9216\nv_mfma_f32_16x16x32_bf16 v[88:91], v[108:111], v[108:111], v[88:91]\n" \
  "ds_read_b128 v[128:131], %[x10] offset:1024\nv_mfma_f32_16x16x32_bf16 v[24:27], v[108:111], v[148:151], v[24:27]\n" \
  "ds_read_b64_tr_b16 v[164:165], %[w2] offset:8192\nv_mfma_f32_16x16x32_bf16 v[40:43], v[112:115], v[148:151], v[40:43]\n" \
  "ds_read_b64_tr_b16 v[166:167], %[w2] offset:9216\nv_mfma_f32_16x16x32_bf16 v[96:99], v[104:107], v[140:143], v[96:99]\n" \
  "ds_read_b128 v[132:135], %[x11] offset:0\nv_mfma_f32_16x16x32_bf16 v[48:51], v[116:119], v[140:143], v[48:51]\n" \
  "ds_read_b64_tr_b16 v[168:169], %[w3] offset:8192\nv_mfma_f32_16x16x32_bf16 v[52:55], v[116:119], v[144:147], v[52:55]\n" \
  "ds_read_b64_tr_b16 v[170:171], %[w3] offset:9216\nv_mfma_f32_16x16x32_bf16 v[100:103], v[104:107], v[144:147], v[100:103]\n" \
  "ds_read_b128 v[136:139], %[x11] offset:1024\nv_mfma_f32_16x16x32_bf16 v[56:59], v[116:119], v[148:151], v[56:59]\nv_add_u32 %[x00], %[dl], %[x00]\n" \
  "v_mfma_f32_16x16x32_bf16 v[28:31], v[108:111], v[152:155], v[28:31]\nv_add_u32 %[w0], %[dl], %[w0]\n" \
  "v_mfma_f32_16x16x32_bf16 v[84:87], v[104:107], v[112:115], v[84:87]\nv_add_u32 %[w1], %[dl], %[w1]\n" \
  "v_mfma_f32_16x16x32_bf16 v[44:47], v[112:115], v[152:155], v[44:47]\nv_add_u32 %[x01], %[dl], %[x01]\n" \
  "v_mfma_f32_16x16x32_bf16 v[60:63], v[116:119], v[152:155], v[60:63]\nv_add_u32 %[w2], %[dl], %[w2]\n" \
  "v_mfma_f32_16x16x32_bf16 v[92:95], v[112:115], v[112:115], v[92:95]\nv_add_u32 %[x10], %[dl], %[x10]\n" \
  "v_mfma_f32_16x16x32_bf16 v[64:67], v[120:123], v[140:143], v[64:67]\nv_add_u32 %[w3], %[dl], %[w3]\n" \
  "v_mfma_f32_16x16x32_bf16 v[68:71], v[120:123], v[144:147], v[68:71]\nv_add_u32 %[x11], %[dl], %[x11]\ns_cmp_lg_u32 %[c], 9\n" \
  "v_mfma_f32_16x16x32_bf16 v[72:75], v[120:123], v[148:151], v[72:75]\ns_waitcnt lgkmcnt(0)\n" \
  "v_mfma_f32_16x16x32_bf16 v[76:79], v[120:123], v[152:155], v[76:79]\ns_cbranch_scc1 1b\n" \
  "v_mfma_f32_16x16x32_bf16 v[88:91], v[124:127], v[124:127], v[88:91]\nv_mfma_f32_16x16x32_bf16 v[92:95], v[128:131], v[128:131], v[92:95]\n" \
  "v_mfma_f32_16x16x32_bf16 v[80:83], v[104:107], v[124:127], v[80:83]\nv_mfma_f32_16x16x32_bf16 v[84:87], v[104:107], v[128:131], v[84:87]\n" \
  "v_cndmask_b32_e64 v108, v88, v89, %[mc1]\nv_cndmask_b32_e64 v109, v90, v91, %[mc1]\n" \
  "v_mfma_f32_16x16x32_bf16 v[96:99], v[104:107], v[156:159], v[96:99]\nv_cndmask_b32_e64 v118, v92, v93, %[mc1]\n" \
  "v_cndmask_b32_e64 v119, v94, v95, %[mc1]\nv_mfma_f32_16x16x32_bf16 v[100:103], v[104:107], v[160:163], v[100:103]\n" \
  "v_cndmask_b32_e64 v110, v108, v109, %[mc2]\nv_cndmask_b32_e64 v111, v118, v119, %[mc2]\n" \
  "v_mfma_f32_16x16x32_bf16 v[16:19], v[124:127], v[156:159], v[16:19]\nv_mul_f32 v112, 0x3acccccd, v80\nv_mul_f32 v113, 0x3acccccd, v84\n" \
  "v_mfma_f32_16x16x32_bf16 v[20:23], v[124:127], v[160:163], v[20:23]\n" \
  "v_pk_fma_f32 v[110:111], v[110:111], %[kc], %[kc] op_sel:[0,0,1] op_sel_hi:[1,0,1]\n" \
  "v_pk_fma_f32 v[110:111], v[112:113], v[112:113], v[110:111] neg_lo:[1,0,0] neg_hi:[1,0,0]\n" \
  "v_mfma_f32_16x16x32_bf16 v[24:27], v[124:127], v[164:167], v[24:27]\nv_max_f32 v110, 0x3727c5ac, v110\nv_max_f32 v111, 0x3727c5ac, v111\n" \
  "v_mfma_f32_16x16x32_bf16 v[28:31], v[124:127], v[168:171], v[28:31]\nv_rsq_f32 v110, v110\nds_write2_b32 %[cw], v96, v100 offset0:0 offset1:16\n" \
  "v_mfma_f32_16x16x32_bf16 v[32:35], v[128:131], v[156:159], v[32:35]\nv_rsq_f32 v111, v111\n" \
  "v_mfma_f32_16x16x32_bf16 v[36:39], v[128:131], v[160:163], v[36:39]\nv_pk_mul_f32 v[114:115], v[110:111], %[qs] op_sel_hi:[1,0]\n" \
  "v_pk_mul_f32 v[116:117], v[112:113], v[114:115] neg_lo:[1,0] neg_hi:[1,0]\nv_mfma_f32_16x16x32_bf16 v[40:43], v[128:131], v[164:167], v[40:43]\n" \
  "ds_write2_b32 %[sw], v114, v116 offset0:0 offset1:128\nds_write2_b32 %[sw], v115, v117 offset0:8 offset1:136\n" \
  "v_mfma_f32_16x16x32_bf16 v[44:47], v[128:131], v[168:171], v[44:47]\nv_mfma_f32_16x16x32_bf16 v[48:51], v[132:135], v[156:159], v[48:51]\n" \
  "v_mfma_f32_16x16x32_bf16 v[52:55], v[132:135], v[160:163], v[52:55]\ns_waitcnt lgkmcnt(0)\ns_barrier\n" \
  "v_mfma_f32_16x16x32_bf16 v[56:59], v[132:135], v[164:167], v[56:59]\nds_read2_b32 v[96:97], %[rc0] offset0:0 offset1:16\n" \
  "ds_read_b128 v[108:111], %[ra0] offset:0\nv_mfma_f32_16x16x32_bf16 v[60:63], v[132:135], v[168:171], v[60:63]\n" \
  "ds_read_b128 v[112:115], %[ra0] offset:512\nds_read_b128 v[116:119], %[ra0] offset:32\n" \
  "v_mfma_f32_16x16x32_bf16 v[64:67], v[136:139], v[156:159], v[64:67]\nds_read_b128 v[120:123], %[ra0] offset:544\n" \
  "ds_read2_b32 v[98:99], %[rc1] offset0:0 offset1:16\nv_mfma_f32_16x16x32_bf16 v[68:71], v[136:139], v[160:163], v[68:71]\n" \
  "ds_read_b128 v[140:143], %[ra1] offset:0\nds_read_b128 v[144:147], %[ra1] offset:512\n" \
  "v_mfma_f32_16x16x32_bf16 v[72:75], v[136:139], v[164:167], v[72:75]\nds_read_b128 v[148:151], %[ra1] offset:32\n" \
  "ds_read_b128 v[152:155], %[ra1] offset:544\nv_mfma_f32_16x16x32_bf16 v[76:79], v[136:139], v[168:171], v[76:79]\ns_waitcnt lgkmcnt(0)\n"
#define QK_STORE \
  "v_pk_mul_f32 v[16:17], v[16:17], v[108:109] op_sel_hi:[1,0]\nv_pk_mul_f32 v[18:19], v[18:19], v[108:109] op_sel_hi:[1,0]\n" \
  "v_pk_mul_f32 v[32:33], v[32:33], v[110:111] op_sel_hi:[1,0]\nv_pk_mul_f32 v[34:35], v[34:35], v[110:111] op_sel_hi:[1,0]\n" \
  "v_pk_fma_f32 v[16:17], v[140:141], v[108:109], v[16:17] op_sel:[0,1,0]\nv_pk_fma_f32 v[18:19], v[142:143], v[108:109], v[18:19] op_sel:[0,1,0]\n" \
  "v_pk_fma_f32 v[32:33], v[140:141], v[110:111], v[32:33] op_sel:[0,1,0]\nv_pk_fma_f32 v[34:35], v[142:143], v[110:111], v[34:35] op_sel:[0,1,0]\n" \
  "v_cvt_pk_bf16_f32 v16, v16, v17\nv_cvt_pk_bf16_f32 v17, v18, v19\nv_cvt_pk_bf16_f32 v18, v32, v33\nv_cvt_pk_bf16_f32 v19, v34, v35\n" \
  "v_pk_mul_f32 v[20:21], v[20:21], v[108:109] op_sel_hi:[1,0]\nv_pk_mul_f32 v[22:23], v[22:23], v[108:109] op_sel_hi:[1,0]\n" \
  "v_pk_mul_f32 v[36:37], v[36:37], v[110:111] op_sel_hi:[1,0]\nv_pk_mul_f32 v[38:39], v[38:39], v[110:111] op_sel_hi:[1,0]\ns_mov_b64 exec, %[m0]\n" \
  "v_permlane16_swap_b32 v16, v18\nv_permlane16_swap_b32 v17, v19\ns_mov_b64 exec, -1\nbuffer_store_dwordx4 v[16:19], %[so0], %[rw], %[b0] offen sc1\n" \
  "v_pk_fma_f32 v[20:21], v[144:145], v[108:109], v[20:21] op_sel:[0,1,0]\nv_pk_fma_f32 v[22:23], v[146:147], v[108:109], v[22:23] op_sel:[0,1,0]\n" \
  "v_pk_fma_f32 v[36:37], v[144:145], v[110:111], v[36:37] op_sel:[0,1,0]\nv_pk_fma_f32 v[38:39], v[146:147], v[110:111], v[38:39] op_sel:[0,1,0]\n" \
  "v_cvt_pk_bf16_f32 v20, v20, v21\nv_cvt_pk_bf16_f32 v21, v22, v23\nv_cvt_pk_bf16_f32 v22, v36, v37\nv_cvt_pk_bf16_f32 v23, v38, v39\n" \
  "v_pk_mul_f32 v[24:25], v[24:25], v[108:109] op_sel_hi:[1,0]\nv_pk_mul_f32 v[26:27], v[26:27], v[108:109] op_sel_hi:[1,0]\n" \
  "v_pk_mul_f32 v[40:41], v[40:41], v[110:111] op_sel_hi:[1,0]\nv_pk_mul_f32 v[42:43], v[42:43], v[110:111] op_sel_hi:[1,0]\ns_mov_b64 exec, %[m1]\n" \
  "v_permlane16_swap_b32 v20, v22\nv_permlane16_swap_b32 v21, v23\ns_mov_b64 exec, -1\nbuffer_store_dwordx4 v[20:23], %[so1], %[rw], %[b0] offen sc1\n" \
  "v_pk_fma_f32 v[24:25], v[148:149], v[108:109], v[24:25] op_sel:[0,1,0]\nv_pk_fma_f32 v[26:27], v[150:151], v[108:109], v[26:27] op_sel:[0,1,0]\n" \
  "v_pk_fma_f32 v[40:41], v[148:149], v[110:111], v[40:41] op_sel:[0,1,0]\nv_pk_fma_f32 v[42:43], v[150:151], v[110:111], v[42:43] op_sel:[0,1,0]\n" \
  "v_cvt_pk_bf16_f32 v24, v24, v25\nv_cvt_pk_bf16_f32 v25, v26, v27\nv_cvt_pk_bf16_f32 v26, v40, v41\nv_cvt_pk_bf16_f32 v27, v42, v43\n" \
  "v_pk_mul_f32 v[28:29], v[28:29], v[108:109] op_sel_hi:[1,0]\nv_pk_mul_f32 v[30:31], v[30:31], v[108:109] op_sel_hi:[1,0]\n" \
  "v_pk_mul_f32 v[44:45], v[44:45], v[110:111] op_sel_hi:[1,0]\nv_pk_mul_f32 v[46:47], v[46:47], v[110:111] op_sel_hi:[1,0]\ns_mov_b64 exec, %[m2]\n" \
  "v_permlane16_swap_b32 v24, v26\nv_permlane16_swap_b32 v25, v27\ns_mov_b64 exec, -1\nbuffer_store_dwordx4 v[24:27], %[so2], %[rw], %[b0] offen sc1\n" \
  "v_pk_fma_f32 v[28:29], v[152:153], v[108:109], v[28:29] op_sel:[0,1,0]\nv_pk_fma_f32 v[30:31], v[154:155], v[108:109], v[30:31] op_sel:[0,1,0]\n" \
  "v_pk_fma_f32 v[44:45], v[152:153], v[110:111], v[44:45] op_sel:[0,1,0]\nv_pk_fma_f32 v[46:47], v[154:155], v[110:111], v[46:47] op_sel:[0,1,0]\n" \
  "v_cvt_pk_bf16_f32 v28, v28, v29\nv_cvt_pk_bf16_f32 v29, v30, v31\nv_cvt_pk_bf16_f32 v30, v44, v45\nv_cvt_pk_bf16_f32 v31, v46, v47\n" \
  "v_pk_mul_f32 v[48:49], v[48:49], v[112:113] op_sel_hi:[1,0]\nv_pk_mul_f32 v[50:51], v[50:51], v[112:113] op_sel_hi:[1,0]\n" \
  "v_pk_mul_f32 v[64:65], v[64:65], v[114:115] op_sel_hi:[1,0]\nv_pk_mul_f32 v[66:67], v[66:67], v[114:115] op_sel_hi:[1,0]\ns_mov_b64 exec, %[m3]\n" \
  "v_permlane16_swap_b32 v28, v30\nv_permlane16_swap_b32 v29, v31\ns_mov_b64 exec, -1\nbuffer_store_dwordx4 v[28:31], %[so3], %[rw], %[b0] offen sc1\n" \
  "v_pk_fma_f32 v[48:49], v[140:141], v[112:113], v[48:49] op_sel:[0,1,0]\nv_pk_fma_f32 v[50:51], v[142:143], v[112:113], v[50:51] op_sel:[0,1,0]\n" \
  "v_pk_fma_f32 v[64:65], v[140:141], v[114:115], v[64:65] op_sel:[0,1,0]\nv_pk_fma_f32 v[66:67], v[142:143], v[114:115], v[66:67] op_sel:[0,1,0]\n" \
  "v_cvt_pk_bf16_f32 v48, v48, v49\nv_cvt_pk_bf16_f32 v49, v50, v51\nv_cvt_pk_bf16_f32 v50, v64, v65\nv_cvt_pk_bf16_f32 v51, v66, v67\n" \
  "v_pk_mul_f32 v[52:53], v[52:53], v[112:113] op_sel_hi:[1,0]\nv_pk_mul_f32 v[54:55], v[54:55], v[112:113] op_sel_hi:[1,0]\n" \
  "v_pk_mul_f32 v[68:69], v[68:69], v[114:115] op_sel_hi:[1,0]\nv_pk_mul_f32 v[70:71], v[70:71], v[114:115] op_sel_hi:[1,0]\ns_mov_b64 exec, %[m0]\n" \
  "v_permlane16_swap_b32 v48, v50\nv_permlane16_swap_b32 v49, v51\ns_mov_b64 exec, -1\nbuffer_store_dwordx4 v[48:51], %[so4], %[rw], %[b0] offen sc1\n" \
  "v_pk_fma_f32 v[52:53], v[144:145], v[112:113], v[52:53] op_sel:[0,1,0]\nv_pk_fma_f32 v[54:55], v[146:147], v[112:113], v[54:55] op_sel:[0,1,0]\n" \
  "v_pk_fma_f32 v[68:69], v[144:145], v[114:115], v[68:69] op_sel:[0,1,0]\nv_pk_fma_f32 v[70:71], v[146:147], v[114:115], v[70:71] op_sel:[0,1,0]\n" \
  "v_cvt_pk_bf16_f32 v52, v52, v53\nv_cvt_pk_bf16_f32 v53, v54, v55\nv_cvt_pk_bf16_f32 v54, v68, v69\nv_cvt_pk_bf16_f32 v55, v70, v71\n" \
  "v_pk_mul_f32 v[56:57], v[56:57], v[112:113] op_sel_hi:[1,0]\nv_pk_mul_f32 v[58:59], v[58:59], v[112:113] op_sel_hi:[1,0]\n" \
  "v_pk_mul_f32 v[72:73], v[72:73], v[114:115] op_sel_hi:[1,0]\nv_pk_mul_f32 v[74:75], v[74:75], v[114:115] op_sel_hi:[1,0]\ns_mov_b64 exec, %[m1]\n" \
  "v_permlane16_swap_b32 v52, v54\nv_permlane16_swap_b32 v53, v55\ns_mov_b64 exec, -1\nbuffer_store_dwordx4 v[52:55], %[so5], %[rw], %[b0] offen sc1\n" \
  "v_pk_fma_f32 v[56:57], v[148:149], v[112:113], v[56:57] op_sel:[0,1,0]\nv_pk_fma_f32 v[58:59], v[150:151], v[112:113], v[58:59] op_sel:[0,1,0]\n" \
  "v_pk_fma_f32 v[72:73], v[148:149], v[114:115], v[72:73] op_sel:[0,1,0]\nv_pk_fma_f32 v[74:75], v[150:151], v[114:115], v[74:75] op_sel:[0,1,0]\n" \
  "v_cvt_pk_bf16_f32 v56, v56, v57\nv_cvt_pk_bf16_f32 v57, v58, v59\nv_cvt_pk_bf16_f32 v58, v72, v73\nv_cvt_pk_bf16_f32 v59, v74, v75\n" \
  "v_pk_mul_f32 v[60:61], v[60:61], v[112:113] op_sel_hi:[1,0]\nv_pk_mul_f32 v[62:63], v[62:63], v[112:113] op_sel_hi:[1,0]\n" \
  "v_pk_mul_f32 v[76:77], v[76:77], v[114:115] op_sel_hi:[1,0]\nv_pk_mul_f32 v[78:79], v[78:79], v[114:115] op_sel_hi:[1,0]\ns_mov_b64 exec, %[m2]\n" \
  "v_permlane16_swap_b32 v56, v58\nv_permlane16_swap_b32 v57, v59\ns_mov_b64 exec, -1\nbuffer_store_dwordx4 v[56:59], %[so6], %[rw], %[b0] offen sc1\n" \
  "v_pk_fma_f32 v[60:61], v[152:153], v[112:113], v[60:61] op_sel:[0,1,0]\nv_pk_fma_f32 v[62:63], v[154:155], v[112:113], v[62:63] op_sel:[0,1,0]\n" \
  "v_pk_fma_f32 v[76:77], v[152:153], v[114:115], v[76:77] op_sel:[0,1,0]\nv_pk_fma_f32 v[78:79], v[154:155], v[114:115], v[78:79] op_sel:[0,1,0]\n" \
  "v_cvt_pk_bf16_f32 v60, v60, v61\nv_cvt_pk_bf16_f32 v61, v62, v63\nv_cvt_pk_bf16_f32 v62, v76, v77\nv_cvt_pk_bf16_f32 v63, v78, v79\ns_nop 0\n" \
  "s_mov_b64 exec, %[m3]\nv_permlane16_swap_b32 v60, v62\nv_permlane16_swap_b32 v61, v63\ns_mov_b64 exec, -1\n" \
  "buffer_store_dwordx4 v[60:63], %[so7], %[rw], %[b0] offen sc1\n"
#define V_STORE \
  "v_pk_mul_f32 v[16:17], v[16:17], v[108:109]\nv_pk_mul_f32 v[18:19], v[18:19], v[110:111]\nv_pk_mul_f32 v[32:33], v[32:33], v[116:117]\n" \
  "v_pk_mul_f32 v[34:35], v[34:35], v[118:119]\nv_pk_fma_f32 v[16:17], v[112:113], v[96:97], v[16:17] op_sel:[0,0,0] op_sel_hi:[1,0,1]\n" \
  "v_pk_fma_f32 v[18:19], v[114:115], v[96:97], v[18:19] op_sel:[0,0,0] op_sel_hi:[1,0,1]\n" \
  "v_pk_fma_f32 v[32:33], v[120:121], v[96:97], v[32:33] op_sel:[0,0,0] op_sel_hi:[1,0,1]\n" \
  "v_pk_fma_f32 v[34:35], v[122:123], v[96:97], v[34:35] op_sel:[0,0,0] op_sel_hi:[1,0,1]\nv_cvt_pk_bf16_f32 v16, v16, v17\n" \
  "v_cvt_pk_bf16_f32 v17, v18, v19\nv_cvt_pk_bf16_f32 v18, v32, v33\nv_cvt_pk_bf16_f32 v19, v34, v35\nv_pk_mul_f32 v[20:21], v[20:21], v[108:109]\n" \
  "v_pk_mul_f32 v[22:23], v[22:23], v[110:111]\nv_pk_mul_f32 v[36:37], v[36:37], v[116:117]\nv_pk_mul_f32 v[38:39], v[38:39], v[118:119]\n" \
  "buffer_store_dwordx4 v[16:19], %[so0], %[rw], %[b0] offen sc1\nv_pk_fma_f32 v[20:21], v[112:113], v[96:97], v[20:21] op_sel:[0,1,0] op_sel_hi:[1,1,1]\n" \
  "v_pk_fma_f32 v[22:23], v[114:115], v[96:97], v[22:23] op_sel:[0,1,0] op_sel_hi:[1,1,1]\n" \
  "v_pk_fma_f32 v[36:37], v[120:121], v[96:97], v[36:37] op_sel:[0,1,0] op_sel_hi:[1,1,1]\n" \
  "v_pk_fma_f32 v[38:39], v[122:123], v[96:97], v[38:39] op_sel:[0,1,0] op_sel_hi:[1,1,1]\nv_cvt_pk_bf16_f32 v20, v20, v21\n" \
  "v_cvt_pk_bf16_f32 v21, v22, v23\nv_cvt_pk_bf16_f32 v22, v36, v37\nv_cvt_pk_bf16_f32 v23, v38, v39\nv_pk_mul_f32 v[24:25], v[24:25], v[108:109]\n" \
  "v_pk_mul_f32 v[26:27], v[26:27], v[110:111]\nv_pk_mul_f32 v[40:41], v[40:41], v[116:117]\nv_pk_mul_f32 v[42:43], v[42:43], v[118:119]\n" \
  "buffer_store_dwordx4 v[20:23], %[so1], %[rw], %[b0] offen sc1\nv_pk_fma_f32 v[24:25], v[112:113], v[98:99], v[24:25] op_sel:[0,0,0] op_sel_hi:[1,0,1]\n" \
  "v_pk_fma_f32 v[26:27], v[114:115], v[98:99], v[26:27] op_sel:[0,0,0] op_sel_hi:[1,0,1]\n" \
  "v_pk_fma_f32 v[40:41], v[120:121], v[98:99], v[40:41] op_sel:[0,0,0] op_sel_hi:[1,0,1]\n" \
  "v_pk_fma_f32 v[42:43], v[122:123], v[98:99], v[42:43] op_sel:[0,0,0] op_sel_hi:[1,0,1]\nv_cvt_pk_bf16_f32 v24, v24, v25\n" \
  "v_cvt_pk_bf16_f32 v25, v26, v27\nv_cvt_pk_bf16_f32 v26, v40, v41\nv_cvt_pk_bf16_f32 v27, v42, v43\nv_pk_mul_f32 v[28:29], v[28:29], v[108:109]\n" \
  "v_pk_mul_f32 v[30:31], v[30:31], v[110:111]\nv_pk_mul_f32 v[44:45], v[44:45], v[116:117]\nv_pk_mul_f32 v[46:47], v[46:47], v[118:119]\n" \
  "buffer_store_dwordx4 v[24:27], %[so2], %[rw], %[b0] offen sc1\nv_pk_fma_f32 v[28:29], v[112:113], v[98:99], v[28:29] op_sel:[0,1,0] op_sel_hi:[1,1,1]\n" \
  "v_pk_fma_f32 v[30:31], v[114:115], v[98:99], v[30:31] op_sel:[0,1,0] op_sel_hi:[1,1,1]\n" \
  "v_pk_fma_f32 v[44:45], v[120:121], v[98:99], v[44:45] op_sel:[0,1,0] op_sel_hi:[1,1,1]\n" \
  "v_pk_fma_f32 v[46:47], v[122:123], v[98:99], v[46:47] op_sel:[0,1,0] op_sel_hi:[1,1,1]\nv_cvt_pk_bf16_f32 v28, v28, v29\n" \
  "v_cvt_pk_bf16_f32 v29, v30, v31\nv_cvt_pk_bf16_f32 v30, v44, v45\nv_cvt_pk_bf16_f32 v31, v46, v47\nv_pk_mul_f32 v[48:49], v[48:49], v[140:141]\n" \
  "v_pk_mul_f32 v[50:51], v[50:51], v[142:143]\nv_pk_mul_f32 v[64:65], v[64:65], v[148:149]\nv_pk_mul_f32 v[66:67], v[66:67], v[150:151]\n" \
  "buffer_store_dwordx4 v[28:31], %[so3], %[rw], %[b0] offen sc1\nv_pk_fma_f32 v[48:49], v[144:145], v[96:97], v[48:49] op_sel:[0,0,0] op_sel_hi:[1,0,1]\n" \
  "v_pk_fma_f32 v[50:51], v[146:147], v[96:97], v[50:51] op_sel:[0,0,0] op_sel_hi:[1,0,1]\n" \
  "v_pk_fma_f32 v[64:65], v[152:153], v[96:97], v[64:65] op_sel:[0,0,0] op_sel_hi:[1,0,1]\n" \
  "v_pk_fma_f32 v[66:67], v[154:155], v[96:97], v[66:67] op_sel:[0,0,0] op_sel_hi:[1,0,1]\nv_cvt_pk_bf16_f32 v48, v48, v49\n" \
  "v_cvt_pk_bf16_f32 v49, v50, v51\nv_cvt_pk_bf16_f32 v50, v64, v65\nv_cvt_pk_bf16_f32 v51, v66, v67\nv_pk_mul_f32 v[52:53], v[52:53], v[140:141]\n" \
  "v_pk_mul_f32 v[54:55], v[54:55], v[142:143]\nv_pk_mul_f32 v[68:69], v[68:69], v[148:149]\nv_pk_mul_f32 v[70:71], v[70:71], v[150:151]\n" \
  "buffer_store_dwordx4 v[48:51], %[so0], %[rw], %[b1] offen sc1\nv_pk_fma_f32 v[52:53], v[144:145], v[96:97], v[52:53] op_sel:[0,1,0] op_sel_hi:[1,1,1]\n" \
  "v_pk_fma_f32 v[54:55], v[146:147], v[96:97], v[54:55] op_sel:[0,1,0] op_sel_hi:[1,1,1]\n" \
  "v_pk_fma_f32 v[68:69], v[152:153], v[96:97], v[68:69] op_sel:[0,1,0] op_sel_hi:[1,1,1]\n" \
  "v_pk_fma_f32 v[70:71], v[154:155], v[96:97], v[70:71] op_sel:[0,1,0] op_sel_hi:[1,1,1]\nv_cvt_pk_bf16_f32 v52, v52, v53\n" \
  "v_cvt_pk_bf16_f32 v53, v54, v55\nv_cvt_pk_bf16_f32 v54, v68, v69\nv_cvt_pk_bf16_f32 v55, v70, v71\nv_pk_mul_f32 v[56:57], v[56:57], v[140:141]\n" \
  "v_pk_mul_f32 v[58:59], v[58:59], v[142:143]\nv_pk_mul_f32 v[72:73], v[72:73], v[148:149]\nv_pk_mul_f32 v[74:75], v[74:75], v[150:151]\n" \
  "buffer_store_dwordx4 v[52:55], %[so1], %[rw], %[b1] offen sc1\nv_pk_fma_f32 v[56:57], v[144:145], v[98:99], v[56:57] op_sel:[0,0,0] op_sel_hi:[1,0,1]\n" \
  "v_pk_fma_f32 v[58:59], v[146:147], v[98:99], v[58:59] op_sel:[0,0,0] op_sel_hi:[1,0,1]\n" \
  "v_pk_fma_f32 v[72:73], v[152:153], v[98:99], v[72:73] op_sel:[0,0,0] op_sel_hi:[1,0,1]\n" \
  "v_pk_fma_f32 v[74:75], v[154:155], v[98:99], v[74:75] op_sel:[0,0,0] op_sel_hi:[1,0,1]\nv_cvt_pk_bf16_f32 v56, v56, v57\n" \
  "v_cvt_pk_bf16_f32 v57, v58, v59\nv_cvt_pk_bf16_f32 v58, v72, v73\nv_cvt_pk_bf16_f32 v59, v74, v75\nv_pk_mul_f32 v[60:61], v[60:61], v[140:141]\n" \
  "v_pk_mul_f32 v[62:63], v[62:63], v[142:143]\nv_pk_mul_f32 v[76:77], v[76:77], v[148:149]\nv_pk_mul_f32 v[78:79], v[78:79], v[150:151]\n" \
  "buffer_store_dwordx4 v[56:59], %[so2], %[rw], %[b1] offen sc1\nv_pk_fma_f32 v[60:61], v[144:145], v[98:99], v[60:61] op_sel:[0,1,0] op_sel_hi:[1,1,1]\n" \
  "v_pk_fma_f32 v[62:63], v[146:147], v[98:99], v[62:63] op_sel:[0,1,0] op_sel_hi:[1,1,1]\n" \
  "v_pk_fma_f32 v[76:77], v[152:153], v[98:99], v[76:77] op_sel:[0,1,0] op_sel_hi:[1,1,1]\n" \
  "v_pk_fma_f32 v[78:79], v[154:155], v[98:99], v[78:79] op_sel:[0,1,0] op_sel_hi:[1,1,1]\nv_cvt_pk_bf16_f32 v60, v60, v61\n" \
  "v_cvt_pk_bf16_f32 v61, v62, v63\nv_cvt_pk_bf16_f32 v62, v76, v77\nv_cvt_pk_bf16_f32 v63, v78, v79\ns_nop 0\n" \
  "buffer_store_dwordx4 v[60:63], %[so3], %[rw], %[b1] offen sc1\n"
template <bool ISV>
DEV void qkv_compute(unsigned l0, int w, int lane, int rt, int ct, char* ws, unsigned junk, bool touch) {
  const int wm = w >> 1, wn = w & 1, c = lane & 15, g = lane >> 4;
  s16x8 ones = {0x3f80, 0x3f80, 0x3f80, 0x3f80, 0x3f80, 0x3f80, 0x3f80, 0x3f80};
  constexpr int XS = ISV ? 1024 : 2048;
  const int xr = ISV ? (c & 7) + 16 * (c >> 3) : c;
  unsigned xa[2][2], wa[4];
#pragma unroll
  for (int k = 0; k < 4; ++k) {
    xa[k >> 1][k & 1] = l0 + (wm * 64 + xr) * 128 + ((g ^ (c >> 1) ^ (4 * (k >> 1))) << 4) + ((k & 1) ^ wn) * 4096;
    const int q = c >> 2, p = c & 3;
    wa[k] = l0 + 16384 + 256 * (8 * g + q) + 16 * ((2 * (4 * wn + (k ^ (2 * wm))) + (p >> 1)) ^ (2 * q + 8 * (g & 1))) + 8 * (p & 1);
  }
  const bool isq = ct < 5;
  const int tile0 = (rt & 1) * 4 + wm * 2;
  unsigned so[ISV ? 4 : 8];
  unsigned long long wide[4];
#pragma unroll
  for (int j = 0; j < 4; ++j) {
    const int cb = wn * 4 + (j ^ (2 * wm));
    if constexpr (!ISV) {
      const int P = (isq ? ct : ct - 5) * 16 + cb * 2 + (g >> 1);
      const int hp = (P * 52429) >> 18, pr = P - 5 * hp;
      const bool hi = pr == 2;
      const unsigned hs = 8 * (isq ? QP : KVP);
      const unsigned lo0 = (tile0 + wn) * 1024 + (g & 1) * 256 + (pr > 2 ? hs - 1536 : 0) + pr * 512, hi0 = 8192 + (tile0 + wn) * 256 + ((g & 1) ? hs : 0);
      so[j] = __umul24(hp, 2 * hs) + c * 16 + (hi ? hi0 : lo0);
      so[4 + j] = so[j] + (hi ? 256 - 512 * wn : 1024 - 2048 * wn);
      wide[j] = __builtin_amdgcn_ballot_w64(!hi);
      asm volatile("" : "+v"(so[j]), "+v"(so[4 + j]));
      asm volatile("" : "+s"(wide[j]));
    } else {
      so[j] = lane * 16 + cb * 1024;
      asm volatile("" : "+v"(so[j]));
    }
  }
  const unsigned stb = l0 + 65536, csb = stb + 1024;
  unsigned cw = csb + (wn * 64 + wm * 32 + c) * 4, sw = stb + (wm * 64 + wn * 32 + xr) * (ISV ? 4 : 8) + (g ^ (c >> 2)) * 2048;
  unsigned ra0 = stb + (wm * 64 + wn * 32 + (ISV ? 16 * (g >> 1) + 4 * (g & 1) : c)) * (ISV ? 4 : 8), ra1 = ra0 ^ (ISV ? 128 : 256);
  unsigned rc0 = csb + (wn * 64 + wm * 32 + (ISV ? c : 4 * g)) * 4, rc1 = rc0 ^ 128;
  const int grp = rt >> 1;
  unsigned base = ISV ? (unsigned)OFF_V + grp * VST + (tile0 * 40 + (ct - 10) * 8) * 1024 : (isq ? (unsigned)OFF_QB : (unsigned)OFF_KV) + grp * (isq ? QP : KVP);
  unsigned long long qs = isq ? 0x3ea52b5bu : 0x3f800000u;
  unsigned b1 = base + (wn ^ 1) * 40960;
  if (ISV) base += wn * 40960;
  asm volatile("" : "+s"(base), "+s"(b1), "+s"(qs), "+s"(ws));
  const rsrc_t rw = mkrs(ws, WS_END);
  asm volatile("" : "+{v[104:107]}"(ones), "+v"(xa[0][0]), "+v"(xa[0][1]), "+v"(xa[1][0]), "+v"(xa[1][1]), "+v"(wa[0]), "+v"(wa[1]), "+v"(wa[2]),
                    "+v"(wa[3]), "+v"(cw), "+v"(sw), "+v"(ra0), "+v"(ra1), "+v"(rc0), "+v"(rc1) : "s"(rw));
  PIN();
  NEWPAGE("k_qkv");
  if (touch) waitv<1>(); else waitv<0>();
  bar();
  STAMPH();
  f32x16 ar[4], e0, e1; f32x4 e2;
  int cnt, dl;
#define QKV_OPS : "=&{v[16:31]}"(ar[0]), "=&{v[32:47]}"(ar[1]), "=&{v[48:63]}"(ar[2]), "=&{v[64:79]}"(ar[3]), "=&{v[108:123]}"(e0), \
    "=&{v[140:155]}"(e1), "=&{v[96:99]}"(e2), [x00] "+v"(xa[0][0]), [x01] "+v"(xa[0][1]), [x10] "+v"(xa[1][0]), [x11] "+v"(xa[1][1]), \
    [w0] "+v"(wa[0]), [w1] "+v"(wa[1]), [w2] "+v"(wa[2]), [w3] "+v"(wa[3]), [c] "=&s"(cnt), [dl] "=&s"(dl) \
  : "{v[104:107]}"(ones), [wrap] "s"(-131072), [cw] "v"(cw), [sw] "v"(sw), [ra0] "v"(ra0), [ra1] "v"(ra1), [rc0] "v"(rc0), [rc1] "v"(rc1), \
    [mc1] "s"(0xaaaaaaaaaaaaaaaaull), [mc2] "s"(0xccccccccccccccccull), [kc] "s"(0x3727c5ac3acccccdull), [qs] "s"(qs) \
  : "v80", "v81", "v82", "v83", "v84", "v85", "v86", "v87", "v88", "v89", "v90", "v91", "v92", "v93", "v94", "v95", "v100", "v101", "v102", \
    "v103", "v124", "v125", "v126", "v127", "v128", "v129", "v130", "v131", "v132", "v133", "v134", "v135", "v136", "v137", "v138", "v139", \
    "v156", "v157", "v158", "v159", "v160", "v161", "v162", "v163", "v164", "v165", "v166", "v167", "v168", "v169", "v170", "v171", "scc", "memory"
  if constexpr (ISV) asm volatile(V_LOOP QKV_OPS); else asm volatile(QK_LOOP QKV_OPS);
  asm volatile("s_waitcnt vmcnt(0)" ::"v"(junk) : "memory");
  PIN();
  STAMP(1, 2);
  STAMPW(1);
  if constexpr (!ISV)
    asm volatile(QK_STORE : "+{v[16:31]}"(ar[0]), "+{v[32:47]}"(ar[1]), "+{v[48:63]}"(ar[2]), "+{v[64:79]}"(ar[3])
                 : "{v[108:123]}"(e0), "{v[140:155]}"(e1), [so0] "v"(so[0]), [so1] "v"(so[1]), [so2] "v"(so[2]), [so3] "v"(so[3]), [so4] "v"(so[4]),
                   [so5] "v"(so[5]), [so6] "v"(so[6]), [so7] "v"(so[7]), [m0] "s"(wide[0]), [m1] "s"(wide[1]), [m2] "s"(wide[2]),
                   [m3] "s"(wide[3]), [rw] "s"(rw), [b0] "s"(base) : "memory");
  else
    asm volatile(V_STORE : "+{v[16:31]}"(ar[0]), "+{v[32:47]}"(ar[1]), "+{v[48:63]}"(ar[2]), "+{v[64:79]}"(ar[3])
                 : "{v[108:123]}"(e0), "{v[140:155]}"(e1), "{v[96:99]}"(e2), [so0] "v"(so[0]), [so1] "v"(so[1]), [so2] "v"(so[2]),
                   [so3] "v"(so[3]), [rw] "s"(rw), [b0] "s"(base), [b1] "s"(b1) : "memory");
}

DEV unsigned warm_touch(int job, int lane) {
  unsigned long long cb; unsigned co, cn;
  asm volatile("s_getpc_b64 %0\n1:\n\ts_mov_b32 %1, k_attn-1b\n\ts_mov_b32 %2, .Lcode_tail-k_attn\n"
               ".subsection 1\n.Lcode_tail:\n.subsection 0" : "=s"(cb), "=s"(co), "=s"(cn));
  typedef const __attribute__((address_space(4))) unsigned long long* kap;
  const unsigned long long ap = ((kap)__builtin_amdgcn_kernarg_segment_ptr())[job < 7 ? job : 7];
  const unsigned n0 = (job == 0 || job == 6) ? S_ * D_ * 2 : D_ * F_ * 2, n1 = job == 1 ? D_ * D_ * 6 : D_ * D_ * 2;
  const unsigned n2 = job < 14 ? (unsigned)WS_END : cn, n = job > 6 ? n2 : (job == 1 || job == 2) ? n1 : n0;
  const int j0 = job < 7 ? job : job < 14 ? 7 : 14;
  const unsigned o = ((job - j0) * 64 + lane) << (job < 14 ? 16 : 7);
  unsigned v;
  asm volatile("buffer_load_dword %0, %1, %2, 0 offen" : "=v"(v) : "v"(min(o, n - 4) & ~3u),
               "s"(mkrs((const void*)(job < 14 ? ap : cb + co), n)) : "memory");
  return v;
}

extern "C" __global__ PAGE __launch_bounds__(512) void k_qkv(const char* x_, const char* wqkv_, const char* wo_, const char* wg_,
                                                        const char* wu_, const char* wd_, const char* out_, char* ws) {
  __shared__ __attribute__((aligned(16))) char lds[163840];
  PAGE_MACROS();
  u64x2 ka;
  asm volatile("s_load_dwordx4 %0, %1, 0x0" : "=s"(ka) : "s"(__builtin_amdgcn_kernarg_segment_ptr()));
  __builtin_amdgcn_sched_barrier(0);
  const int bid = blockIdx.x, xcd = bid & 7, loc = bid >> 3;
  const int rt = 4 * (xcd >> 1) + (loc & 3), ct = 8 * (xcd & 1) + (loc >> 2);
  STAMP(1, 0);
  const int lane = threadIdx.x & 63, w = __builtin_amdgcn_readfirstlane(threadIdx.x >> 6);
  if (ct == 15) return;
  const unsigned l0 = (unsigned)(size_t)(LDS_AS char*)lds;
#define K1ROT 0x2020
#define K1XO 0x27946150
  int rot = ((K1ROT >> (4 * ((loc + (loc >> 2)) & 3))) & 15) + ((K1XO >> (4 * xcd)) & 15);
  if (rot >= 10) rot -= 10;
  const int i = w & 3, lq = lane >> 4;
  const bool isv = ct >= 10;
  const unsigned vx = __umul24(lane >> 3, 1280) + (((lane & 7) ^ lq) << 4), vw = __umul24(lq, 3840) + (((lane & 15) ^ (2 * lq)) << 4);
  const char *x, *wqkv;
  {
    const unsigned od = (w & 1) << 6;
    unsigned a0 = vx ^ (isv ? od : 0), a1 = (vx ^ (isv ? od : 64)) + 9216, b0 = vw ^ (2 * od), b1 = b0 + 14336;
    unsigned ox = (rt * 128 + 16 * w) * 1280 + rot * 128, ow = ct * 256 + (rot * 64 + 8 * w) * 3840, lm = l0 + w * 2048;
    setm0(lm);
    asm volatile("s_waitcnt lgkmcnt(0)" : "+s"(ka), "+v"(a0), "+v"(a1), "+v"(b0), "+v"(b1), "+s"(ox), "+s"(ow), "+s"(lm));
    x = (const char*)ka[0]; wqkv = (const char*)ka[1];
    gldso<0>(x + ox, a0); gldso<1024>(x + ox, a1);
    setm0(lm + 16384);
    gldso<0>(wqkv + ow, b0); gldso<1024>(wqkv + ow, b1);
  }
  bar();
  if (__builtin_expect(w < 4, 1)) {
    const unsigned lb = l0 + i * 4096;
    unsigned xv[4] = {vx, (vx ^ (isv ? 0 : 64)) + 9216, (vx ^ (isv ? 64 : 0)) + 2 * 9216, (vx ^ 64) + 3 * 9216};
    unsigned wv[4] = {vw, vw + 14336, (vw ^ 128) + 2 * 14336, (vw ^ 128) + 3 * 14336};
    const char* xb = x + (unsigned)((rt * 128 + 32 * i) * 1280);
    const char* wb = wqkv + (unsigned)(ct * 256 + 16 * i * 3840);
    auto issue = [&](int s) {
      const int q = s + rot >= 10 ? s + rot - 10 : s + rot;
      const char* xs = xb + q * 128;
      const char* wq = wb + q * (64 * 3840);
      setm0(lb + (s % 5) * 32768);
      gldso<0>(xs, xv[0]); gldso<1024>(xs, xv[1]); gldso<2048>(xs, xv[2]); gldso<3072>(xs, xv[3]);
      setm0(lb + (s % 5) * 32768 + 16384);
      gldso<0>(wq, wv[0]); gldso<1024>(wq, wv[1]); gldso<2048>(wq, wv[2]); gldso<3072>(wq, wv[3]);
    };
    sfor<K1S0 == 1 ? 0 : 1, K1PRE>([&](auto J) { issue(J.v); });
    sfor<0, 10>([&](auto I) {
      constexpr auto cnt = [](int b) { return cmin(10, cmin(K1PRE + K1RAMP * b, cmax(5, b + K1RING))); };
      constexpr int s = I.v, n0 = cnt(s), n1 = cnt(s + 1);
      waitv<(n0 - s - 1) * 8>();
      bar();
      sfor<n0, n1>([&](auto J) { issue(J.v); });
    });
    VISIT("k_attn", w, 0x1f);
    if (ct == 10 && !(rt & 1) && w == 0 && lane < 8) {
      const unsigned one = lane ? 0u : 0x3f803f80u;
      bst16<16>(u32x4{one, one, one, one}, mkrs(ws, WS_END), lane * 16, (unsigned)OFF_V + (rt >> 1) * VST + VC);
    }
    return;
  }
  unsigned junk = 0;
  const bool touch = w == 4 && loc < 24;
  if (touch) junk = warm_touch(loc, lane);
  if (ct < 10) qkv_compute<false>(l0, w - 4, lane, rt, ct, ws, junk, touch);
  else qkv_compute<true>(l0, w - 4, lane, rt, ct, ws, junk, touch);
  STAMP(1, 3);
  STAMPE(1, 4);
}

DEV void attn_exp(const f32x16& s, s16x8& p0, s16x8& p1) {
  u32x4 a, b;
#pragma unroll
  for (int e = 0; e < 4; ++e) {
    a[e] = pk2(__builtin_amdgcn_exp2f(s[2 * e]), __builtin_amdgcn_exp2f(s[2 * e + 1]));
    b[e] = pk2(__builtin_amdgcn_exp2f(s[8 + 2 * e]), __builtin_amdgcn_exp2f(s[8 + 2 * e + 1]));
  }
  p0 = __builtin_bit_cast(s16x8, a); p1 = __builtin_bit_cast(s16x8, b);
}
DEV float max16(const f32x16& s) {
  float m = __builtin_fmaxf(s[0], s[1]);
#pragma unroll
  for (int e = 2; e < 16; ++e) m = __builtin_fmaxf(m, s[e]);
  return xmax32(m);
}

#define ATTN_LOOP \
  "v_mov_b32 v172, 0\nv_mov_b32 v173, 0\nv_mov_b32 v174, 0\nv_mov_b32 v175, 0\nv_mov_b32 v160, 0\nv_mov_b32 v161, 0\nv_mov_b32 v162, 0\n" \
  "v_mov_b32 v163, 0\ns_mov_b32 %[c], 0\nv_mfma_f32_32x32x8_bf16 v[80:95], v[144:145], v[128:129], 0\n" \
  "v_mfma_f32_32x32x16_bf16 v[80:95], v[136:139], v[112:115], v[80:95]\ns_nop 7\ns_nop 3\n1:\n" \
  "v_mfma_f32_32x32x8_bf16 v[96:111], v[144:145], v[130:131], 0\nv_exp_f32 v164, v80\nv_exp_f32 v176, v81\nv_exp_f32 v165, v82\nv_exp_f32 v177, v83\n" \
  "v_cvt_pk_bf16_f32 v164, v164, v176\nv_cvt_pk_bf16_f32 v165, v165, v177\nv_mfma_f32_32x32x16_bf16 v[96:111], v[136:139], v[116:119], v[96:111]\n" \
  "v_exp_f32 v166, v84\nv_exp_f32 v176, v85\nv_exp_f32 v167, v86\nv_exp_f32 v177, v87\nv_cvt_pk_bf16_f32 v166, v166, v176\n" \
  "v_cvt_pk_bf16_f32 v167, v167, v177\nv_mfma_f32_32x32x16_bf16 v[64:79], v[160:163], v[172:175], v[64:79]\nv_exp_f32 v168, v88\nv_exp_f32 v176, v89\n" \
  "v_exp_f32 v169, v90\nv_exp_f32 v177, v91\nv_cvt_pk_bf16_f32 v168, v168, v176\nv_cvt_pk_bf16_f32 v169, v169, v177\n" \
  "v_mfma_f32_32x32x16_bf16 v[16:31], v[148:151], v[164:167], v[16:31]\nds_read_b64 v[146:147], %[ha] offset:256\nv_exp_f32 v170, v92\n" \
  "v_exp_f32 v176, v93\nv_exp_f32 v171, v94\nv_exp_f32 v177, v95\nv_cvt_pk_bf16_f32 v170, v170, v176\nv_cvt_pk_bf16_f32 v171, v171, v177\n" \
  "v_mfma_f32_32x32x8_bf16 v[80:95], v[144:145], v[132:133], 0\nv_exp_f32 v164, v96\nv_exp_f32 v176, v97\nv_exp_f32 v165, v98\nv_exp_f32 v177, v99\n" \
  "v_cvt_pk_bf16_f32 v164, v164, v176\nv_cvt_pk_bf16_f32 v165, v165, v177\nv_mfma_f32_32x32x16_bf16 v[80:95], v[136:139], v[120:123], v[80:95]\n" \
  "ds_read_b128 v[140:143], %[ka] offset:1024\nv_exp_f32 v166, v100\nv_exp_f32 v176, v101\nv_exp_f32 v167, v102\nv_exp_f32 v177, v103\n" \
  "v_cvt_pk_bf16_f32 v166, v166, v176\nv_cvt_pk_bf16_f32 v167, v167, v177\nv_mfma_f32_32x32x16_bf16 v[16:31], v[156:159], v[168:171], v[16:31]\n" \
  "v_exp_f32 v172, v104\nv_exp_f32 v176, v105\nv_exp_f32 v173, v106\nv_exp_f32 v177, v107\nv_cvt_pk_bf16_f32 v172, v172, v176\n" \
  "v_cvt_pk_bf16_f32 v173, v173, v177\nv_mfma_f32_32x32x16_bf16 v[32:47], v[148:151], v[164:167], v[32:47]\nds_read_b128 v[152:155], %[va] offset:1408\n" \
  "v_exp_f32 v174, v108\nv_exp_f32 v176, v109\nv_exp_f32 v175, v110\nv_exp_f32 v177, v111\nv_cvt_pk_bf16_f32 v174, v174, v176\n" \
  "v_cvt_pk_bf16_f32 v175, v175, v177\nv_mfma_f32_32x32x8_bf16 v[96:111], v[144:145], v[134:135], 0\nv_exp_f32 v164, v80\nv_exp_f32 v176, v81\n" \
  "v_exp_f32 v165, v82\nv_exp_f32 v177, v83\nv_cvt_pk_bf16_f32 v164, v164, v176\nv_cvt_pk_bf16_f32 v165, v165, v177\n" \
  "v_mfma_f32_32x32x16_bf16 v[96:111], v[136:139], v[124:127], v[96:111]\nds_read_b128 v[160:163], %[va] offset:2112\nv_exp_f32 v166, v84\n" \
  "v_exp_f32 v176, v85\nv_exp_f32 v167, v86\nv_exp_f32 v177, v87\nv_cvt_pk_bf16_f32 v166, v166, v176\nv_cvt_pk_bf16_f32 v167, v167, v177\n" \
  "v_mfma_f32_32x32x16_bf16 v[32:47], v[156:159], v[172:175], v[32:47]\nv_exp_f32 v168, v88\nv_exp_f32 v176, v89\nv_exp_f32 v169, v90\n" \
  "v_exp_f32 v177, v91\nv_cvt_pk_bf16_f32 v168, v168, v176\nv_cvt_pk_bf16_f32 v169, v169, v177\n" \
  "v_mfma_f32_32x32x16_bf16 v[48:63], v[148:151], v[164:167], v[48:63]\nv_exp_f32 v170, v92\nv_exp_f32 v176, v93\nv_exp_f32 v171, v94\n" \
  "v_exp_f32 v177, v95\nv_cvt_pk_bf16_f32 v170, v170, v176\nv_cvt_pk_bf16_f32 v171, v171, v177\ns_waitcnt lgkmcnt(0)\n" \
  "v_mfma_f32_32x32x8_bf16 v[80:95], v[146:147], v[128:129], 0\nv_exp_f32 v164, v96\nv_exp_f32 v176, v97\nv_exp_f32 v165, v98\nv_exp_f32 v177, v99\n" \
  "v_cvt_pk_bf16_f32 v164, v164, v176\nv_cvt_pk_bf16_f32 v165, v165, v177\nv_mfma_f32_32x32x16_bf16 v[80:95], v[140:143], v[112:115], v[80:95]\n" \
  "v_exp_f32 v166, v100\nv_exp_f32 v176, v101\nv_exp_f32 v167, v102\nv_exp_f32 v177, v103\nv_cvt_pk_bf16_f32 v166, v166, v176\n" \
  "v_cvt_pk_bf16_f32 v167, v167, v177\nv_mfma_f32_32x32x16_bf16 v[48:63], v[156:159], v[168:171], v[48:63]\nv_exp_f32 v172, v104\nv_exp_f32 v176, v105\n" \
  "v_exp_f32 v173, v106\nv_exp_f32 v177, v107\nv_cvt_pk_bf16_f32 v172, v172, v176\nv_cvt_pk_bf16_f32 v173, v173, v177\n" \
  "v_mfma_f32_32x32x16_bf16 v[64:79], v[148:151], v[164:167], v[64:79]\nv_exp_f32 v174, v108\nv_exp_f32 v176, v109\nv_exp_f32 v175, v110\n" \
  "v_exp_f32 v177, v111\nv_cvt_pk_bf16_f32 v174, v174, v176\nv_cvt_pk_bf16_f32 v175, v175, v177\n" \
  "v_mfma_f32_32x32x8_bf16 v[96:111], v[146:147], v[130:131], 0\nv_exp_f32 v164, v80\nv_exp_f32 v176, v81\nv_exp_f32 v165, v82\nv_exp_f32 v177, v83\n" \
  "v_cvt_pk_bf16_f32 v164, v164, v176\nv_cvt_pk_bf16_f32 v165, v165, v177\nv_mfma_f32_32x32x16_bf16 v[96:111], v[140:143], v[116:119], v[96:111]\n" \
  "v_exp_f32 v166, v84\nv_exp_f32 v176, v85\nv_exp_f32 v167, v86\nv_exp_f32 v177, v87\nv_cvt_pk_bf16_f32 v166, v166, v176\n" \
  "v_cvt_pk_bf16_f32 v167, v167, v177\nv_mfma_f32_32x32x16_bf16 v[64:79], v[156:159], v[172:175], v[64:79]\nv_exp_f32 v168, v88\nv_exp_f32 v176, v89\n" \
  "v_exp_f32 v169, v90\nv_exp_f32 v177, v91\nv_cvt_pk_bf16_f32 v168, v168, v176\nv_cvt_pk_bf16_f32 v169, v169, v177\n" \
  "v_mfma_f32_32x32x16_bf16 v[16:31], v[152:155], v[164:167], v[16:31]\nds_read_b64 v[144:145], %[ha] offset:512\nv_exp_f32 v170, v92\n" \
  "v_exp_f32 v176, v93\nv_exp_f32 v171, v94\nv_exp_f32 v177, v95\nv_cvt_pk_bf16_f32 v170, v170, v176\nv_cvt_pk_bf16_f32 v171, v171, v177\n" \
  "v_mfma_f32_32x32x8_bf16 v[80:95], v[146:147], v[132:133], 0\nv_exp_f32 v164, v96\nv_exp_f32 v176, v97\nv_exp_f32 v165, v98\nv_exp_f32 v177, v99\n" \
  "v_cvt_pk_bf16_f32 v164, v164, v176\nv_cvt_pk_bf16_f32 v165, v165, v177\nv_mfma_f32_32x32x16_bf16 v[80:95], v[140:143], v[120:123], v[80:95]\n" \
  "ds_read_b128 v[136:139], %[ka] offset:2048\nv_exp_f32 v166, v100\nv_exp_f32 v176, v101\nv_exp_f32 v167, v102\nv_exp_f32 v177, v103\n" \
  "v_cvt_pk_bf16_f32 v166, v166, v176\nv_cvt_pk_bf16_f32 v167, v167, v177\nv_mfma_f32_32x32x16_bf16 v[16:31], v[160:163], v[168:171], v[16:31]\n" \
  "v_exp_f32 v172, v104\nv_exp_f32 v176, v105\nv_exp_f32 v173, v106\nv_exp_f32 v177, v107\nv_cvt_pk_bf16_f32 v172, v172, v176\n" \
  "v_cvt_pk_bf16_f32 v173, v173, v177\nv_mfma_f32_32x32x16_bf16 v[32:47], v[152:155], v[164:167], v[32:47]\nds_read_b128 v[148:151], %[va] offset:2816\n" \
  "v_exp_f32 v174, v108\nv_exp_f32 v176, v109\nv_exp_f32 v175, v110\nv_exp_f32 v177, v111\nv_cvt_pk_bf16_f32 v174, v174, v176\n" \
  "v_cvt_pk_bf16_f32 v175, v175, v177\nv_mfma_f32_32x32x8_bf16 v[96:111], v[146:147], v[134:135], 0\nv_exp_f32 v164, v80\nv_exp_f32 v176, v81\n" \
  "v_exp_f32 v165, v82\nv_exp_f32 v177, v83\nv_cvt_pk_bf16_f32 v164, v164, v176\nv_cvt_pk_bf16_f32 v165, v165, v177\n" \
  "v_mfma_f32_32x32x16_bf16 v[96:111], v[140:143], v[124:127], v[96:111]\nds_read_b128 v[156:159], %[va] offset:3520\nv_exp_f32 v166, v84\n" \
  "v_exp_f32 v176, v85\nv_exp_f32 v167, v86\nv_exp_f32 v177, v87\nv_cvt_pk_bf16_f32 v166, v166, v176\nv_cvt_pk_bf16_f32 v167, v167, v177\n" \
  "v_mfma_f32_32x32x16_bf16 v[32:47], v[160:163], v[172:175], v[32:47]\nv_exp_f32 v168, v88\nv_exp_f32 v176, v89\nv_exp_f32 v169, v90\n" \
  "v_exp_f32 v177, v91\nv_cvt_pk_bf16_f32 v168, v168, v176\nv_cvt_pk_bf16_f32 v169, v169, v177\n" \
  "v_mfma_f32_32x32x16_bf16 v[48:63], v[152:155], v[164:167], v[48:63]\nv_exp_f32 v170, v92\nv_exp_f32 v176, v93\nv_exp_f32 v171, v94\n" \
  "v_exp_f32 v177, v95\nv_cvt_pk_bf16_f32 v170, v170, v176\nv_cvt_pk_bf16_f32 v171, v171, v177\ns_waitcnt lgkmcnt(0)\n" \
  "v_mfma_f32_32x32x8_bf16 v[80:95], v[144:145], v[128:129], 0\nv_exp_f32 v164, v96\nv_exp_f32 v176, v97\nv_exp_f32 v165, v98\nv_exp_f32 v177, v99\n" \
  "v_cvt_pk_bf16_f32 v164, v164, v176\nv_cvt_pk_bf16_f32 v165, v165, v177\nv_mfma_f32_32x32x16_bf16 v[80:95], v[136:139], v[112:115], v[80:95]\n" \
  "v_exp_f32 v166, v100\nv_exp_f32 v176, v101\nv_exp_f32 v167, v102\nv_exp_f32 v177, v103\nv_cvt_pk_bf16_f32 v166, v166, v176\n" \
  "v_cvt_pk_bf16_f32 v167, v167, v177\nv_mfma_f32_32x32x16_bf16 v[48:63], v[160:163], v[168:171], v[48:63]\nv_exp_f32 v172, v104\nv_exp_f32 v176, v105\n" \
  "v_exp_f32 v173, v106\nv_exp_f32 v177, v107\nv_cvt_pk_bf16_f32 v172, v172, v176\nv_cvt_pk_bf16_f32 v173, v173, v177\n" \
  "v_mfma_f32_32x32x16_bf16 v[64:79], v[152:155], v[164:167], v[64:79]\nv_exp_f32 v174, v108\nv_exp_f32 v176, v109\nv_exp_f32 v175, v110\n" \
  "v_exp_f32 v177, v111\nv_cvt_pk_bf16_f32 v174, v174, v176\nv_cvt_pk_bf16_f32 v175, v175, v177\ns_bitcmp1_b32 0x43, %[c]\ns_cbranch_scc0 2f\ns_barrier\n" \
  "2:\nv_mfma_f32_32x32x8_bf16 v[96:111], v[144:145], v[130:131], 0\nv_exp_f32 v164, v80\nv_exp_f32 v176, v81\nv_exp_f32 v165, v82\nv_exp_f32 v177, v83\n" \
  "v_cvt_pk_bf16_f32 v164, v164, v176\nv_cvt_pk_bf16_f32 v165, v165, v177\nv_mfma_f32_32x32x16_bf16 v[96:111], v[136:139], v[116:119], v[96:111]\n" \
  "s_cmp_eq_u32 %[c], 6\ns_cselect_b32 %[dl], %[wrap], %[kvp]\nv_exp_f32 v166, v84\nv_exp_f32 v176, v85\nv_exp_f32 v167, v86\nv_exp_f32 v177, v87\n" \
  "v_cvt_pk_bf16_f32 v166, v166, v176\nv_cvt_pk_bf16_f32 v167, v167, v177\nv_mfma_f32_32x32x16_bf16 v[64:79], v[160:163], v[172:175], v[64:79]\n" \
  "v_exp_f32 v168, v88\nv_exp_f32 v176, v89\nv_exp_f32 v169, v90\nv_exp_f32 v177, v91\nv_cvt_pk_bf16_f32 v168, v168, v176\n" \
  "v_cvt_pk_bf16_f32 v169, v169, v177\nv_mfma_f32_32x32x16_bf16 v[16:31], v[148:151], v[164:167], v[16:31]\nds_read_b64 v[146:147], %[ha] offset:768\n" \
  "v_exp_f32 v170, v92\nv_exp_f32 v176, v93\nv_exp_f32 v171, v94\nv_exp_f32 v177, v95\nv_cvt_pk_bf16_f32 v170, v170, v176\n" \
  "v_cvt_pk_bf16_f32 v171, v171, v177\nv_mfma_f32_32x32x8_bf16 v[80:95], v[144:145], v[132:133], 0\nv_exp_f32 v164, v96\nv_exp_f32 v176, v97\n" \
  "v_exp_f32 v165, v98\nv_exp_f32 v177, v99\nv_cvt_pk_bf16_f32 v164, v164, v176\nv_cvt_pk_bf16_f32 v165, v165, v177\n" \
  "v_mfma_f32_32x32x16_bf16 v[80:95], v[136:139], v[120:123], v[80:95]\nds_read_b128 v[140:143], %[ka] offset:3072\nv_exp_f32 v166, v100\n" \
  "v_exp_f32 v176, v101\nv_exp_f32 v167, v102\nv_exp_f32 v177, v103\nv_cvt_pk_bf16_f32 v166, v166, v176\nv_cvt_pk_bf16_f32 v167, v167, v177\n" \
  "v_mfma_f32_32x32x16_bf16 v[16:31], v[156:159], v[168:171], v[16:31]\nv_exp_f32 v172, v104\nv_exp_f32 v176, v105\nv_exp_f32 v173, v106\n" \
  "v_exp_f32 v177, v107\nv_cvt_pk_bf16_f32 v172, v172, v176\nv_cvt_pk_bf16_f32 v173, v173, v177\n" \
  "v_mfma_f32_32x32x16_bf16 v[32:47], v[148:151], v[164:167], v[32:47]\nds_read_b128 v[152:155], %[va] offset:4224\nv_exp_f32 v174, v108\n" \
  "v_exp_f32 v176, v109\nv_exp_f32 v175, v110\nv_exp_f32 v177, v111\nv_cvt_pk_bf16_f32 v174, v174, v176\nv_cvt_pk_bf16_f32 v175, v175, v177\n" \
  "v_mfma_f32_32x32x8_bf16 v[96:111], v[144:145], v[134:135], 0\nv_exp_f32 v164, v80\nv_exp_f32 v176, v81\nv_exp_f32 v165, v82\nv_exp_f32 v177, v83\n" \
  "v_cvt_pk_bf16_f32 v164, v164, v176\nv_cvt_pk_bf16_f32 v165, v165, v177\nv_mfma_f32_32x32x16_bf16 v[96:111], v[136:139], v[124:127], v[96:111]\n" \
  "ds_read_b128 v[160:163], %[va] offset:4928\nv_exp_f32 v166, v84\nv_exp_f32 v176, v85\nv_exp_f32 v167, v86\nv_exp_f32 v177, v87\n" \
  "v_cvt_pk_bf16_f32 v166, v166, v176\nv_cvt_pk_bf16_f32 v167, v167, v177\nv_mfma_f32_32x32x16_bf16 v[32:47], v[156:159], v[172:175], v[32:47]\n" \
  "v_exp_f32 v168, v88\nv_exp_f32 v176, v89\nv_exp_f32 v169, v90\nv_exp_f32 v177, v91\nv_cvt_pk_bf16_f32 v168, v168, v176\n" \
  "v_cvt_pk_bf16_f32 v169, v169, v177\nv_mfma_f32_32x32x16_bf16 v[48:63], v[148:151], v[164:167], v[48:63]\nv_exp_f32 v170, v92\nv_exp_f32 v176, v93\n" \
  "v_exp_f32 v171, v94\nv_exp_f32 v177, v95\nv_cvt_pk_bf16_f32 v170, v170, v176\nv_cvt_pk_bf16_f32 v171, v171, v177\ns_waitcnt lgkmcnt(0)\n" \
  "v_mfma_f32_32x32x8_bf16 v[80:95], v[146:147], v[128:129], 0\nv_exp_f32 v164, v96\nv_exp_f32 v176, v97\nv_exp_f32 v165, v98\nv_exp_f32 v177, v99\n" \
  "v_cvt_pk_bf16_f32 v164, v164, v176\nv_cvt_pk_bf16_f32 v165, v165, v177\nv_mfma_f32_32x32x16_bf16 v[80:95], v[140:143], v[112:115], v[80:95]\n" \
  "v_exp_f32 v166, v100\nv_exp_f32 v176, v101\nv_exp_f32 v167, v102\nv_exp_f32 v177, v103\nv_cvt_pk_bf16_f32 v166, v166, v176\n" \
  "v_cvt_pk_bf16_f32 v167, v167, v177\nv_mfma_f32_32x32x16_bf16 v[48:63], v[156:159], v[168:171], v[48:63]\nv_exp_f32 v172, v104\nv_exp_f32 v176, v105\n" \
  "v_exp_f32 v173, v106\nv_exp_f32 v177, v107\nv_cvt_pk_bf16_f32 v172, v172, v176\nv_cvt_pk_bf16_f32 v173, v173, v177\n" \
  "v_mfma_f32_32x32x16_bf16 v[64:79], v[148:151], v[164:167], v[64:79]\nv_exp_f32 v174, v108\nv_exp_f32 v176, v109\nv_exp_f32 v175, v110\n" \
  "v_exp_f32 v177, v111\nv_cvt_pk_bf16_f32 v174, v174, v176\nv_cvt_pk_bf16_f32 v175, v175, v177\n" \
  "v_mfma_f32_32x32x8_bf16 v[96:111], v[146:147], v[130:131], 0\nv_add_u32 %[ka], %[dl], %[ka]\nv_exp_f32 v164, v80\nv_exp_f32 v176, v81\n" \
  "v_exp_f32 v165, v82\nv_exp_f32 v177, v83\nv_cvt_pk_bf16_f32 v164, v164, v176\nv_cvt_pk_bf16_f32 v165, v165, v177\n" \
  "v_mfma_f32_32x32x16_bf16 v[96:111], v[140:143], v[116:119], v[96:111]\nv_mad_i32_i24 %[ha], %[hm], %[dl], %[ha]\nv_exp_f32 v166, v84\n" \
  "v_exp_f32 v176, v85\nv_exp_f32 v167, v86\nv_exp_f32 v177, v87\nv_cvt_pk_bf16_f32 v166, v166, v176\nv_cvt_pk_bf16_f32 v167, v167, v177\n" \
  "v_mfma_f32_32x32x16_bf16 v[64:79], v[156:159], v[172:175], v[64:79]\nv_add_u32 %[va], %[dl], %[va]\nv_exp_f32 v168, v88\nv_exp_f32 v176, v89\n" \
  "v_exp_f32 v169, v90\nv_exp_f32 v177, v91\nv_cvt_pk_bf16_f32 v168, v168, v176\nv_cvt_pk_bf16_f32 v169, v169, v177\n" \
  "v_mfma_f32_32x32x16_bf16 v[16:31], v[152:155], v[164:167], v[16:31]\nds_read_b64 v[144:145], %[ha] offset:0\nv_exp_f32 v170, v92\n" \
  "v_exp_f32 v176, v93\nv_exp_f32 v171, v94\nv_exp_f32 v177, v95\nv_cvt_pk_bf16_f32 v170, v170, v176\nv_cvt_pk_bf16_f32 v171, v171, v177\n" \
  "v_mfma_f32_32x32x8_bf16 v[80:95], v[146:147], v[132:133], 0\nv_exp_f32 v164, v96\nv_exp_f32 v176, v97\nv_exp_f32 v165, v98\nv_exp_f32 v177, v99\n" \
  "v_cvt_pk_bf16_f32 v164, v164, v176\nv_cvt_pk_bf16_f32 v165, v165, v177\nv_mfma_f32_32x32x16_bf16 v[80:95], v[140:143], v[120:123], v[80:95]\n" \
  "ds_read_b128 v[136:139], %[ka] offset:0\nv_exp_f32 v166, v100\nv_exp_f32 v176, v101\nv_exp_f32 v167, v102\nv_exp_f32 v177, v103\n" \
  "v_cvt_pk_bf16_f32 v166, v166, v176\nv_cvt_pk_bf16_f32 v167, v167, v177\nv_mfma_f32_32x32x16_bf16 v[16:31], v[160:163], v[168:171], v[16:31]\n" \
  "v_exp_f32 v172, v104\nv_exp_f32 v176, v105\nv_exp_f32 v173, v106\nv_exp_f32 v177, v107\nv_cvt_pk_bf16_f32 v172, v172, v176\n" \
  "v_cvt_pk_bf16_f32 v173, v173, v177\nv_mfma_f32_32x32x16_bf16 v[32:47], v[152:155], v[164:167], v[32:47]\nds_read_b128 v[148:151], %[va] offset:0\n" \
  "v_exp_f32 v174, v108\nv_exp_f32 v176, v109\nv_exp_f32 v175, v110\nv_exp_f32 v177, v111\nv_cvt_pk_bf16_f32 v174, v174, v176\n" \
  "v_cvt_pk_bf16_f32 v175, v175, v177\nv_mfma_f32_32x32x8_bf16 v[96:111], v[146:147], v[134:135], 0\nv_exp_f32 v164, v80\nv_exp_f32 v176, v81\n" \
  "v_exp_f32 v165, v82\nv_exp_f32 v177, v83\nv_cvt_pk_bf16_f32 v164, v164, v176\nv_cvt_pk_bf16_f32 v165, v165, v177\n" \
  "v_mfma_f32_32x32x16_bf16 v[96:111], v[140:143], v[124:127], v[96:111]\nds_read_b128 v[156:159], %[va] offset:704\nv_exp_f32 v166, v84\n" \
  "v_exp_f32 v176, v85\nv_exp_f32 v167, v86\nv_exp_f32 v177, v87\nv_cvt_pk_bf16_f32 v166, v166, v176\nv_cvt_pk_bf16_f32 v167, v167, v177\n" \
  "v_mfma_f32_32x32x16_bf16 v[32:47], v[160:163], v[172:175], v[32:47]\nv_exp_f32 v168, v88\nv_exp_f32 v176, v89\nv_exp_f32 v169, v90\n" \
  "v_exp_f32 v177, v91\nv_cvt_pk_bf16_f32 v168, v168, v176\nv_cvt_pk_bf16_f32 v169, v169, v177\n" \
  "v_mfma_f32_32x32x16_bf16 v[48:63], v[152:155], v[164:167], v[48:63]\nv_exp_f32 v170, v92\nv_exp_f32 v176, v93\nv_exp_f32 v171, v94\n" \
  "v_exp_f32 v177, v95\nv_cvt_pk_bf16_f32 v170, v170, v176\nv_cvt_pk_bf16_f32 v171, v171, v177\ns_waitcnt lgkmcnt(0)\n" \
  "v_mfma_f32_32x32x8_bf16 v[80:95], v[144:145], v[128:129], 0\nv_exp_f32 v164, v96\nv_exp_f32 v176, v97\nv_exp_f32 v165, v98\nv_exp_f32 v177, v99\n" \
  "v_cvt_pk_bf16_f32 v164, v164, v176\nv_cvt_pk_bf16_f32 v165, v165, v177\nv_mfma_f32_32x32x16_bf16 v[80:95], v[136:139], v[112:115], v[80:95]\n" \
  "s_add_u32 %[c], %[c], 1\nv_exp_f32 v166, v100\nv_exp_f32 v176, v101\nv_exp_f32 v167, v102\nv_exp_f32 v177, v103\nv_cvt_pk_bf16_f32 v166, v166, v176\n" \
  "v_cvt_pk_bf16_f32 v167, v167, v177\nv_mfma_f32_32x32x16_bf16 v[48:63], v[160:163], v[168:171], v[48:63]\nv_exp_f32 v172, v104\nv_exp_f32 v176, v105\n" \
  "v_exp_f32 v173, v106\nv_exp_f32 v177, v107\nv_cvt_pk_bf16_f32 v172, v172, v176\nv_cvt_pk_bf16_f32 v173, v173, v177\n" \
  "v_mfma_f32_32x32x16_bf16 v[64:79], v[152:155], v[164:167], v[64:79]\ns_cmp_lg_u32 %[c], 8\nv_exp_f32 v174, v108\nv_exp_f32 v176, v109\n" \
  "v_exp_f32 v175, v110\nv_exp_f32 v177, v111\nv_cvt_pk_bf16_f32 v174, v174, v176\nv_cvt_pk_bf16_f32 v175, v175, v177\ns_cbranch_scc1 1b\ns_nop 1\n" \
  "v_mfma_f32_32x32x16_bf16 v[64:79], v[160:163], v[172:175], v[64:79]\ns_waitcnt lgkmcnt(0)\ns_nop 7\ns_nop 7\n"

DEV f32x16 attn_slow(const char* qb, int qt, const char* kv, const char* vg, int head, int lane) {
  const int r = lane & 31, h = lane >> 5, rh = (r & 15) * 16 + (r >> 4) * 8;
  f32x16 z, o;
#pragma unroll
  for (int e = 0; e < 16; ++e) z[e] = 0.f;
  float m = -__builtin_inff();
  o = z;
  const int col = head * 20 + r;
  const unsigned gv = r < 20 ? (col >> 4) * 1024 + h * 256 + (col & 15) * 16 : VC + (r > 20) * 16, gt = r < 20 ? 40960 : 0, g2 = r < 20 ? 512 : 0;
  const s16x8 qlo = *(const s16x8*)(qb + qt * 1024 + lane * 16);
  s16x4 qhi = *(const s16x4*)(qb + 8192 + qt * 256 + rh);
  if (h) qhi = s16x4{0, 0, 0, 0};
  for (int t = 0; t < 64; ++t) {
    const char* b = kv + (t >> 3) * KVP; const int tl = t & 7;
    f32x16 s = MFMA32(*(const s16x8*)(b + tl * 1024 + lane * 16), qlo,
                      MFMA32H(*(const s16x4*)(b + 8192 + tl * 256 + rh), qhi, z));
    const float mn = __builtin_fmaxf(m, max16(s));
    const float al = __builtin_amdgcn_exp2f(m - mn);
    m = mn;
#pragma unroll
    for (int e = 0; e < 16; ++e) { s[e] -= mn; o[e] *= al; }
    s16x8 p0, p1;
    attn_exp(s, p0, p1);
    const char* v = vg + (t >> 3) * VST + (gv + tl * gt);
    o = MFMA32(*(const s16x8*)v, p0, o);
    o = MFMA32(*(const s16x8*)(v + g2), p1, o);
  }
  return o;
}
DEV float hi32(float x) {
  auto r = __builtin_amdgcn_permlane32_swap(__float_as_uint(x), __float_as_uint(x), false, false);
  return __uint_as_float(r[1]);
}

extern "C" __global__ PAGE __launch_bounds__(512) void k_attn(char* ws, const unsigned short* wo, const unsigned short* wg,
                                              const unsigned short* wu, const unsigned short* wd) {
  __shared__ __attribute__((aligned(16))) char lds[7 * KVP + QP + 1024];
  PAD("k_attn", 0);
  const int bid = blockIdx.x, head = 4 * (bid & 7) + ((bid >> 3) & 3), qg = bid >> 5;
  const int lane = threadIdx.x & 63, w = __builtin_amdgcn_readfirstlane(threadIdx.x >> 6);
  const int r = lane & 31, h = lane >> 5;
  const unsigned l0 = (unsigned)(size_t)(LDS_AS char*)lds;
  const char *kv, *vg;
  STAMP(2, 0);
  constexpr unsigned QOFF = 7 * KVP, COFF = QOFF + QP;
  const unsigned l16 = lane * 16;
  auto vof = [&](int pc) {
    const int t = pc * 64 + lane, tl = __umul24(t, 745) >> 16, rm = t - 88 * tl, g4 = __umul24(rm, 47) >> 10, d = rm - 22 * g4, col = head * 20 + d;
    unsigned a = __umul24(tl, 40960) + (col >> 4) * 1024 + g4 * 256 + (col & 15) * 16, b = VC - 320 + d * 16;
    asm volatile("" : "+v"(a), "+v"(b));
    return d < 20 ? a : b;
  };
  {
    const int pd = min(w + 4, 10);
    unsigned vc = vof(w & 3), vd = vof(pd);
    vc = w < 4 ? l16 : vc;
    unsigned qo = (unsigned)OFF_QB + (head * 8 + qg) * QP + w * 1024, ko = (unsigned)OFF_KV + head * (8 * KVP) + w * 1024, lm = l0 + w * 1024;
    unsigned o1 = w < 2 ? qo + 8192 : ko - 2048, m1 = w < 2 ? lm + QOFF + 8192 : lm - 2048, o2 = w < 4 ? ko + 6144 : (unsigned)OFF_V;
    asm volatile("" : "+v"(vc), "+v"(vd), "+s"(qo), "+s"(o1), "+s"(m1), "+s"(o2), "+s"(lm));
    __builtin_amdgcn_sched_barrier(0);
    asm volatile("" : "+s"(ws));
    kv = ws + OFF_KV + (size_t)head * (8 * KVP); vg = ws + OFF_V;
    setm0(lm + QOFF); gldso<0>(ws + qo, l16);
    setm0(m1); gldso<0>(ws + o1, l16);
    setm0(lm + 6144); gldso<0>(ws + o2, vc);
    setm0(l0 + (pd + 10) * 1024); gldso<0>(vg, vd);
  }
  if (__builtin_expect(w < 4, 1)) {
    PAD("k_attn", 1);
    const int p2 = min(w + 8, 10);
    const unsigned v0 = w * 1024 + l16, v1 = (w & 1) * 1024 + l16, vv[3] = {vof(w), vof(w + 4), vof(p2)};
    auto issue = [&](int c, int slot) {
      const char* b = kv + c * KVP; const char* v = vg + c * VST; const unsigned sb = l0 + slot * KVP;
      glds(b, v0, sb + w * 1024); glds(b + 4096, v0, sb + 4096 + w * 1024); glds(b + 8192, v1, sb + 8192 + (w & 1) * 1024);
      glds(v, vv[0], sb + 10240 + w * 1024); glds(v, vv[1], sb + 14336 + w * 1024); glds(v, vv[2], sb + 10240 + p2 * 1024);
    };
    issue(1, 1);
    waitv<6>(); bar();
#pragma unroll
    for (int c = 2; c < 7; ++c) issue(c, c);
    waitv<30>(); bar();
    waitv<0>();
    STAMP(2, 1);
    bar();
    STAMP(2, 2);
    issue(7, 0);
    if (bid * 4 + w < 910) prep_job(bid * 4 + w, wo, wg, wu, wd, ws, lane);
    waitv<0>();
    STAMP(2, 3);
    bar();
    {
      const unsigned id = ((bid >> 3) * 4 + w) * 64 + lane, xh = bid & 1;
      unsigned j0, j1, j2 = 0;
      const unsigned p0 = (unsigned)OFF_WO + xh * 409600 + min(id, 3199u) * 128;
      const unsigned p1 = (unsigned)OFF_WGU + (xh * 8 + (id & 7)) * (20 * 14336) + (id >> 3) * 128;
      asm volatile("global_load_dword %0, %2, %4\n\tglobal_load_dword %1, %3, %4"
                   : "=&v"(j0), "=&v"(j1) : "v"(p0), "v"(p1), "s"(ws) : "memory");
      asm volatile("global_load_dword %0, %1, %2" : "=&v"(j2) : "v"(p1), "s"(ws + 131072) : "memory");
      asm volatile("s_waitcnt vmcnt(0)" ::"v"(j0), "v"(j1), "v"(j2) : "memory");
    }
    VISIT("k_oproj", w, 0x0f);
    VISIT("k_gu", w, 0x3f);
    STAMP(2, 4);
    return;
  }
  PAD("k_attn", 2);
  const int wq = w - 4, qh = wq & 1, kh = wq >> 1;
  unsigned ka = l0 + kh * 4096 + lane * 16;
  const int rh = (r & 15) * 16 + (r >> 4) * 8;
  unsigned ha = h ? l0 + COFF : l0 + 8192 + kh * 1024 + rh;
  unsigned va = l0 + 10240 + kh * 5632 + h * 352 + (r <= ZROW ? r : ZROW) * 16;
  const int hm = 1 - h;
  if (lane < 4) ldsw<u32x2>(l0 + COFF + lane * 256, u32x2{0x3f803f80u, 0u});
  f32x16 z, o0, o1, o2, o3;
#pragma unroll
  for (int e = 0; e < 16; ++e) z[e] = 0.f;
  o0 = z; o1 = z; o2 = z; o3 = z;
  asm volatile("" : "+{v[16:31]}"(o0), "+{v[32:47]}"(o1), "+{v[48:63]}"(o2), "+{v[64:79]}"(o3));
  const int qt = qh * 4 + kh * 2;
  unsigned qa[2], qc[2], so[2];
#pragma unroll
  for (int i = 0; i < 2; ++i) {
    qa[i] = l0 + QOFF + (qt ^ (2 * i)) * 1024 + lane * 16;
    qc[i] = l0 + QOFF + 8192 + (qt ^ (2 * i)) * 256 + rh;
  }
  so[0] = (r >> 4) * 20480 + (r & 15) * 16 + ((head >> 1) * 5 + (head & 1) * 2 + h) * 256;
  so[1] = (r >> 4) * 20480 + (r & 15) * 16 + ((head >> 1) * 5 + 4) * 256 + (head & 1) * 8;
  unsigned xw = l0 + KVP + wq * 6144 + lane * 16, xr = l0 + KVP + (wq ^ 2) * 6144 + lane * 16;
  asm volatile("" : "+v"(qa[0]), "+v"(qa[1]), "+v"(qc[0]), "+v"(qc[1]), "+v"(so[0]), "+v"(so[1]), "+v"(xw), "+v"(xr));
  NEWPAGE("k_attn");
  PAD("k_attn", 4);
  waitv<0>();
  bar();
  STAMP(2, 1);
  s16x8 ql[4]; u32x2 qx[4];
  const s16x8 k00 = ldsr<s16x8>(l0 + lane * 16);
  const s16x4 h00 = ldsr<s16x4>(l0 + 8192 + rh);
#pragma unroll
  for (int i = 0; i < 4; ++i) {
    ql[i] = ldsr<s16x8>(qa[i >> 1] + (i & 1) * 1024);
    qx[i] = ldsr<u32x2>(qc[i >> 1] + (i & 1) * 256);
  }
  s16x8 kl = ldsr<s16x8>(ka), vA = ldsr<s16x8>(va), vB = ldsr<s16x8>(va + 704);
  s16x4 kx = ldsr<s16x4>(ha);
  f32x16 s0[4];
#pragma unroll
  for (int i = 0; i < 4; ++i) if (h) qx[i] = u32x2{0u, 0u};
  asm volatile("s_nop 1" : "+v"(qx[0]), "+v"(qx[1]), "+v"(qx[2]), "+v"(qx[3]));
#pragma unroll
  for (int i = 0; i < 4; ++i) {
    asm volatile("v_mfma_f32_32x32x8_bf16 %0, %1, %2, 0" : "=&v"(s0[i]) : "v"(h00), "v"(qx[i]));
    mfma32_acc(s0[i], k00, ql[i]);
  }
  asm volatile("s_nop 2" : "+v"(s0[0]), "+v"(s0[1]), "+v"(s0[2]));
#pragma unroll
  for (int i = 0; i < 4; ++i) {
    if (i == 3) asm volatile("" : "+v"(s0[3]), "+v"(qx[2]));
    const float nm = -max16(s0[i]);
    const unsigned sh = pk2(nm, nm - bflo(pk2(nm, nm)));
    if (h) qx[i][0] = sh;
  }
  int cc, dl;
  asm volatile(ATTN_LOOP
               : "+{v[16:31]}"(o0), "+{v[32:47]}"(o1), "+{v[48:63]}"(o2), "+{v[64:79]}"(o3), "+{v[136:139]}"(kl), "+{v[144:145]}"(kx),
                 "+{v[148:151]}"(vA), "+{v[156:159]}"(vB), [ka] "+v"(ka), [ha] "+v"(ha), [va] "+v"(va), [c] "=&s"(cc), [dl] "=&s"(dl)
               : "{v[112:115]}"(ql[0]), "{v[116:119]}"(ql[1]), "{v[120:123]}"(ql[2]), "{v[124:127]}"(ql[3]), "{v[128:129]}"(qx[0]),
                 "{v[130:131]}"(qx[1]), "{v[132:133]}"(qx[2]), "{v[134:135]}"(qx[3]), [hm] "v"(hm), [kvp] "s"(KVP), [wrap] "s"(-6 * KVP)
               : "v80", "v81", "v82", "v83", "v84", "v85", "v86", "v87", "v88", "v89", "v90", "v91", "v92", "v93", "v94", "v95", "v96", "v97",
                 "v98", "v99", "v100", "v101", "v102", "v103", "v104", "v105", "v106", "v107", "v108", "v109", "v110", "v111", "v140", "v141",
                 "v142", "v143", "v146", "v147", "v152", "v153", "v154", "v155", "v160", "v161", "v162", "v163", "v164", "v165", "v166", "v167",
                 "v168", "v169", "v170", "v171", "v172", "v173", "v174", "v175", "v176", "v177", "scc", "memory");
  STAMP(2, 2);
#pragma unroll
  for (int u = 0; u < 3; ++u) {
    ldsw<f32x4>(xw + u * 1024, f32x4{o2[4 * u], o2[4 * u + 1], o2[4 * u + 2], o2[4 * u + 3]});
    ldsw<f32x4>(xw + 3072 + u * 1024, f32x4{o3[4 * u], o3[4 * u + 1], o3[4 * u + 2], o3[4 * u + 3]});
  }
  lbar();
  float chk = 0.f;
#pragma unroll
  for (int u = 0; u < 3; ++u) {
    const f32x4 pa = ldsr<f32x4>(xr + u * 1024), pb = ldsr<f32x4>(xr + 3072 + u * 1024);
#pragma unroll
    for (int e = 0; e < 4; ++e) { o0[4 * u + e] += pa[e]; o1[4 * u + e] += pb[e]; chk += o0[4 * u + e] + o1[4 * u + e]; PADHERE("k_attn"); }
  }
  float l0_ = hi32(o0[8]), l1_ = hi32(o1[8]);
  const bool bad = !(__builtin_fmaxf(l0_, l1_) < 1.8446744e19f) || !(__builtin_fabsf(chk) < 3.0e38f);
  if (__builtin_expect(__builtin_amdgcn_ballot_w64(bad) != 0, 0)) {
    const char* qb = ws + OFF_QB + (size_t)(head * 8 + qg) * QP;
    for (int i = 0; i < 2; ++i) { o0 = o1; o1 = attn_slow(qb, qt + i, kv, vg, head, lane); }
    l0_ = hi32(o0[8]); l1_ = hi32(o1[8]);
  }
  PADHERE("k_attn");
  const float i0 = __builtin_amdgcn_rcpf(l0_), i1 = __builtin_amdgcn_rcpf(l1_);
  const rsrc_t rw = mkrs(ws, WS_END);
  const unsigned ob = (unsigned)OFF_OB + (qg * 16 + qt * 2) * 20480;
  PADHERE("k_attn");
  bst16<K2A>(u32x4{pk2(o0[0] * i0, o0[1] * i0), pk2(o0[2] * i0, o0[3] * i0), pk2(o0[4] * i0, o0[5] * i0), pk2(o0[6] * i0, o0[7] * i0)}, rw, so[0], ob);
  PADHERE("k_attn");
  bst16<K2A>(u32x4{pk2(o1[0] * i1, o1[1] * i1), pk2(o1[2] * i1, o1[3] * i1), pk2(o1[4] * i1, o1[5] * i1), pk2(o1[6] * i1, o1[7] * i1)}, rw, so[0], ob + 40960);
  PADHERE("k_attn");
  if (h == 0) {
    bst8(u32x2{pk2(o0[8] * i0, o0[9] * i0), pk2(o0[10] * i0, o0[11] * i0)}, rw, so[1], ob);
    bst8(u32x2{pk2(o1[8] * i1, o1[9] * i1), pk2(o1[10] * i1, o1[11] * i1)}, rw, so[1], ob + 40960);
  }
  STAMP(2, 3);
  STAMPE(2, 4);
}

template <int MODE> struct GC;
template <> struct GC<0> { static constexpr int TT = 1, NT = 5, KB = 20, KPS = 4, SLOTS = 5, XD = 20, XI = 8, PD = 10; };
template <> struct GC<2> { static constexpr int TT = 1, NT = 5, KB = 54, KPS = 4, SLOTS = 7, XD = 16, XI = 8, PD = 10; };

template <int MODE>
DEV void gemm_body(const char* xin, char* ws, char* out) {
  using C = GC<MODE>;
  constexpr int TT = C::TT, NT = C::NT, KB = C::KB, KPS = C::KPS, SLOTS = C::SLOTS, XD = C::XD, XI = C::XI, PD = C::PD;
  constexpr int NS = (KB + KPS - 1) / KPS, SLOT = NT * KPS * 1024, PER = NT * KPS / 4;
  constexpr int NF = KB * NT;
#define G5ILV 1
  constexpr bool ILV = MODE == 2 && G5ILV;
  __shared__ __attribute__((aligned(16))) char lds[SLOTS * SLOT];
  if constexpr (MODE == 0) PAD("k_oproj", 0); else PAD("k_down", 0);
  const int bid = blockIdx.x, xcd = bid & 7, loc = bid >> 3;
  int tb, cg;
  tb = 8 * (xcd >> 1) + (loc & 7); cg = 4 * (xcd & 1) + (loc >> 3);
  const int lane = threadIdx.x & 63, w = __builtin_amdgcn_readfirstlane(threadIdx.x >> 6);
  const unsigned l0 = (unsigned)(size_t)(LDS_AS char*)lds;
  STAMP(3 + MODE, 0);
  if (__builtin_expect(w < 4, 1)) {
    static_assert(PER == NT, "one k-block per loader wave and stage");
    if constexpr (MODE == 0) PAD("k_oproj", 1); else PAD("k_down", 1);
    const char* wb = ws + (MODE == 0 ? OFF_WO : OFF_WD) + (cg * KB + (ILV ? (w & 1) * (KB / 2) + (w >> 1) : w)) * (NT * 1024);
    const unsigned vo = lane * 16, lb = l0 + w * (PER * 1024);
    static_assert(PER == 5, "");
    const unsigned v4 = vo + 1024;
    auto issue = [&](int s) {
      const char* g = wb + s * (ILV ? SLOT / 2 : SLOT);
      const unsigned m = lb + (s % SLOTS) * SLOT;
      setm0(m);
      sfor<0, 4>([&](auto J) { gldso<J.v * 1024>(g, vo); if constexpr (MODE == 2) PADHERE("k_down"); });
      setm0(m + 1024);
      gldso<3072>(g, v4);
      if constexpr (MODE == 2) PADHERE("k_down");
    };
    issue(0); issue(1);
    sfor<0, NS>([&](auto I) {
      constexpr int s = I.v, n0 = ring_issued(s, NS, SLOTS, 2), n1 = ring_issued(s + 1, NS, SLOTS, 2);
      waitv<(n0 - s - 1) * PER>();
      bar();
      sfor<n0, n1>([&](auto J) { issue(J.v); });
    });
    if (MODE == 0 && bid < 16) {
      const int idx = threadIdx.x, ti = idx >> 4, n = ti >= 7, i = ti - 7 * n;
      int ps = (i < 4 ? 4 * n + i : i < 6 ? 4 + 2 * n + i : 12 + n) + 2 * (bid & 1);
      if (ps >= 14) ps -= 14;
      const int t = min(14 * bid + ps, 215);
      const unsigned cv = (((idx >> 1) & 1) * F_ + 64 * (t >> 3) + 32 * (t & 1) + 8 * ((idx >> 2) & 3) + 4 * (idx & 1) + ((t & 7) >> 1)) * 4;
      const rsrc_t rw = mkrs(ws, WS_END);
      float cp[10];
#pragma unroll
      for (int k = 0; k < 10; ++k) cp[k] = __uint_as_float(__builtin_amdgcn_raw_buffer_load_b32(rw, cv, (unsigned)OFF_CSP + k * (2 * F_ * 4), 0));
      const float cs = ((cp[0] + cp[1]) + (cp[2] + cp[3])) + ((cp[4] + cp[5]) + (cp[6] + cp[7])) + (cp[8] + cp[9]);
      __builtin_amdgcn_raw_buffer_store_b32(__float_as_uint(cs), rw, idx * 4, (unsigned)OFF_CS2 + bid * 1024, 0);
    }
    return;
  }
  if constexpr (MODE == 0) PAD("k_oproj", 2); else PAD("k_down", 2);
  const int tok16 = lane & 15, g = lane >> 4;
  const int tt0 = (tb * 4 + w - 4) * TT;
  const rsrc_t rw = mkrs(ws, WS_END);
  const unsigned xb = (unsigned)(MODE == 0 ? OFF_OB : OFF_MB) + tt0 * (KB * 1024);
  const unsigned l16 = lane * 16;
  s16x8 xr[XD][TT];
  auto ldx = [&](auto K) {
    xr[K.v % XD][0] = __builtin_bit_cast(s16x8, bld16(rw, l16, xb + (ILV ? (K.v & 1) * (KB / 2) + (K.v >> 1) : K.v) * 1024));
  };
  sfor<0, XI>(ldx);
  PIN();
  u32x2 res[NT];
  unsigned ro = MODE == 0 ? (tt0 * 16 + tok16) * 1280 + (cg * 80 + 4 * g) * 2 : (tt0 * 16 + (tok16 & 7)) * 1280 + cg * 128 + (tok16 >> 3) * 64 + g * 16;
  unsigned r4 = (tt0 * 16 + tok16) * 1280 + 1024 + cg * 32 + g * 8;
  if constexpr (MODE == 2) asm volatile("" : "+v"(ro), "+v"(r4));
  if constexpr (MODE == 0) {
    const rsrc_t rx = mkrs(xin, S_ * D_ * 2);
#pragma unroll
    for (int n = 0; n < NT; ++n) res[n] = bld8(rx, ro + n * 32, 0);
  } else if constexpr (MODE == 2) {
    const unsigned hv = (((2 * g) & 3) * 16 + tok16) * 16 + (g >> 1) * 8, hb = (unsigned)OFF_HB + tt0 * 20480;
#pragma unroll
    for (int n = 0; n < 4; ++n) res[n] = bld8(rw, hv, hb + (2 * cg + (n >> 1)) * 1024 + (n & 1) * 256);
    res[4] = bld8(rw, l16, hb + (16 + (cg >> 1)) * 1024 + (cg & 1) * 8);
  }
  f32x4 acc[TT][NT];
  s16x8 wf[PD + 1];
  unsigned la[3] = {l0 + l16, l0 + l16 + 65536, l0 + l16 + 131072};
  asm volatile("" : "+v"(la[1]), "+v"(la[2]));
  auto rd = [&](auto I) {
    constexpr int i = I.v, kb = i / NT, n = i % NT;
    if constexpr (i > 0 && i % (KPS * NT) == 0) bar();
    constexpr int o = ((kb / KPS) % SLOTS) * SLOT + ((kb % KPS) * NT + n) * 1024;
    wf[i % (PD + 1)] = ldsr<s16x8>(la[o >> 16] + (o & 65535));
  };
  if constexpr (MODE == 0) { NEWPAGE("k_oproj"); PAD("k_oproj", 3); }
  lbar();
  STAMP(3 + MODE, 1);
  sfor<0, PD>(rd);
  PIN();
  sfor<0, NF>([&](auto I) {
    constexpr int i = I.v, kb = i / NT, n = i % NT;
#pragma unroll
    for (int t = 0; t < TT; ++t) {
      if constexpr (kb == 0) mfma16_new(acc[t][n], wf[i % (PD + 1)], xr[kb % XD][t]);
      else mfma16_acc(acc[t][n], wf[i % (PD + 1)], xr[kb % XD][t]);
      if constexpr (MODE == 2) PADHERE("k_down");
      if constexpr (i + PD < NF) { if (t == 0) rd(IC<i + PD>{}); }
    }
    if constexpr (n == NT - 1) {
      sfor<XI, KB>([&](auto K) {
        if constexpr (cmax(K.v - XD, (K.v - XI) >> 1) == kb) ldx(K);
      });
    }
    PIN();
  });
  asm volatile("s_nop 7\n\ts_nop 7" ::: "memory");
  PIN();
  STAMP(3 + MODE, 2);
  if constexpr (MODE == 0) {
    float s1 = 0.f, s2 = 0.f;
    u32x2 v[NT];
#pragma unroll
    for (int n = 0; n < NT; ++n) {
      v[n][0] = pk2(acc[0][n][0] + bflo(res[n][0]), acc[0][n][1] + bfhi(res[n][0]));
      v[n][1] = pk2(acc[0][n][2] + bflo(res[n][1]), acc[0][n][3] + bfhi(res[n][1]));
    }
    const unsigned hb = (unsigned)OFF_HB + (tt0 * 20 + ((cg * 5) >> 1)) * 1024;
    if (cg & 1) {
      bst16<K3A>(u32x4{v[1][0], v[1][1], v[2][0], v[2][1]}, rw, l16, hb + 1024);
      bst16<K3A>(u32x4{v[3][0], v[3][1], v[4][0], v[4][1]}, rw, l16, hb + 2048);
      bst8(v[0], rw, l16, hb + 8);
    } else {
      bst16<K3A>(u32x4{v[0][0], v[0][1], v[1][0], v[1][1]}, rw, l16, hb);
      bst16<K3A>(u32x4{v[2][0], v[2][1], v[3][0], v[3][1]}, rw, l16, hb + 1024);
      bst8(v[4], rw, l16, hb + 2048);
    }
#pragma unroll
    for (int n = 0; n < NT; ++n) {
      s1 = dot2(v[n][0], 0x3f803f80u, s1); s1 = dot2(v[n][1], 0x3f803f80u, s1);
      s2 = dot2(v[n][0], v[n][0], s2); s2 = dot2(v[n][1], v[n][1], s2);
    }
    s1 = xsum32(xsum16(s1)); s2 = xsum32(xsum16(s2));
    if (g == 0) bst8(u32x2{__float_as_uint(s1), __float_as_uint(s2)}, rw, tok16 * 32, (unsigned)OFF_ST + tt0 * 1024 + (cg >> 2) * 512 + (cg & 3) * 8);
  } else if constexpr (MODE == 2) {
    const rsrc_t ry = mkrs(out, S_ * D_ * 2);
    unsigned v[NT][2];
#pragma unroll
    for (int n = 0; n < NT; ++n) {
      v[n][0] = pk2(acc[0][n][0] + bflo(res[n][0]), acc[0][n][1] + bfhi(res[n][0]));
      v[n][1] = pk2(acc[0][n][2] + bflo(res[n][1]), acc[0][n][3] + bfhi(res[n][1]));
    }
    u32x4 lo, hi;
#pragma unroll
    for (int e = 0; e < 4; ++e) {
      lo[e] = __builtin_amdgcn_update_dpp(v[e >> 1][e & 1], v[2 + (e >> 1)][e & 1], 0x128, 0xf, 0xc, false);
      hi[e] = __builtin_amdgcn_update_dpp(v[2 + (e >> 1)][e & 1], v[e >> 1][e & 1], 0x128, 0xf, 0x3, false);
    }
    bst16<OUTAUX>(lo, ry, ro, 0);
    bst16<OUTAUX>(hi, ry, ro, 8 * 1280);
    bst8(u32x2{v[4][0], v[4][1]}, ry, r4, 0);
  }
  STAMP(3 + MODE, 3);
  STAMPE(3 + MODE, 4);
}

extern "C" __global__ PAGE __launch_bounds__(512) void k_oproj(const char* xin, char* ws) { gemm_body<0>(xin, ws, nullptr); }

extern "C" __global__ PAGE __launch_bounds__(512) void k_gu(char* ws) {
  constexpr int SLOT = 22528, CSO = 7 * SLOT, Q0 = 280, QS = 196;
  __shared__ __attribute__((aligned(16))) char lds[CSO + 1024];
  PAD("k_gu", 0);
  const int bid = blockIdx.x, xcd = bid & 7, loc = bid >> 3;
  const int tb = 4 * (xcd >> 1) + (loc & 3), cg = 8 * (xcd & 1) + (loc >> 2);
  const int lane = threadIdx.x & 63, w = __builtin_amdgcn_readfirstlane(threadIdx.x >> 6);
  const unsigned l0 = (unsigned)(size_t)(LDS_AS char*)lds, l16 = lane * 16;
  const unsigned wb = (unsigned)OFF_WGU + cg * (20 * 14336), xb = (unsigned)OFF_HB + tb * (8 * 20480);
  const rsrc_t rw = mkrs(ws, WS_END);
  STAMP(4, 0);
  auto wo = [&](int p) { return wb + ((cg == 15 && p > 3 && p != 6 && p != 13) ? 0 : p * 1024); };
  auto pw = [&](int s, int p) { setm0(l0 + s * SLOT + p * 1024); gldso<0>(ws + (wo(p) + s * 14336), l16); };
  auto px = [&](int s, int t) { setm0(l0 + s * SLOT + 14336 + t * 1024); gldso<0>(ws + (xb + s * 1024 + t * 20480), l16); };
  pw(0, w); px(0, w); pw(0, 8 + min(w, 5));
  if (__builtin_expect(w < 4, 1)) {
    PAD("k_gu", 1);
    pw(1, w); px(1, w); pw(1, w + 4); px(1, w + 4); pw(1, w + 8); pw(1, 12 + (w & 1));
    const int e = w & 1, hi = w >> 1;
    unsigned vo[11];
#pragma unroll
    for (int k = 0; k < 11; ++k) {
      const unsigned a0 = k < 3 ? wo(k) : k < 6 ? wo(k + 4) : k == 6 ? xb + 7 * 20480 : wo(k - 4), a1 = k < 7 ? xb + k * 20480 : wo(k + 3);
      vo[k] = l16 + (e ? a1 : a0) - (k < 6 ? k % 3 : k == 6 ? 0 : k - 7) * 1024;
    }
    const unsigned st2 = e ? 1024 : 14336;
    const unsigned lb[4] = {l0 + e * 14336, l0 + 7168 + e * 10240, l0 + 21504 - e * 1024, l0 + 3072 + e * 7168};
    const char* gw = ws + hi * 14336;
    const char* gx = ws + hi * 1024;
    const char* ge = ws + hi * st2;
    auto issue = [&](auto B, auto E) {
      sfor<B.v, E.v>([&](auto I) {
        constexpr int s = 2 + 2 * (I.v / 11), k = I.v % 11, gr = k < 6 ? k / 3 : k == 6 ? 2 : 3, im = k < 6 ? k % 3 : k == 6 ? 0 : k - 7;
        if constexpr (im == 0 || I.v == B.v) setm0(lb[gr] + (hi ? (s + 1) % 7 : s % 7) * SLOT);
        gldso<im * 1024>(gr < 2 ? ge + s * st2 : gr == 2 ? gx + s * 1024 : gw + s * 14336, vo[k]);
        PADHERE("k_gu");
      });
    };
    issue(IC<0>{}, IC<6>{});
    waitv<12>(); bar();
    issue(IC<6>{}, IC<11>{});
    waitv<11>(); bar();
    issue(IC<11>{}, IC<18>{});
    waitv<7>(); bar();
    setm0(l0 + CSO);
    gldso<0>(ws + (OFF_CS2 + cg * 1024), l16);
    issue(IC<18>{}, IC<33>{});
    waitv<11>(); bar();
    sfor<3, 6>([&](auto D) {
      issue(IC<11 * D.v>{}, IC<11 * D.v + 11>{});
      waitv<11>(); bar();
    });
    issue(IC<66>{}, IC<77>{});
    waitv<11>(); bar();
    issue(IC<77>{}, IC<84>{});
    waitv<7>(); bar();
    issue(IC<88>{}, IC<95>{}); issue(IC<84>{}, IC<88>{}); issue(IC<95>{}, IC<99>{});
    waitv<15>(); bar();
    waitv<8>(); bar();
    waitv<0>(); bar();
    VISIT("k_down", w, 0x1f);
    return;
  }
  PAD("k_gu", 2);
  const int wq = w - 4, wm = wq >> 1, wn = wq & 1, g = lane >> 4, tt0 = tb * 8 + wm * 4;
  f32x4 acc[28], c4[2];
  s16x8 wf[3], xf[4][4];
  float nmean[4], vr[4], nk[4];
  f32x4 rs[4];
  unsigned aw[3], ax[3];
  {
    const int gu = (lane >> 1) & 1;
    aw[0] = l0 + wn * 7168 + gu * 512 + (g ^ gu) * 128 + ((lane >> 2) & 3) * 32 + (lane & 1) * 16;
    ax[0] = l0 + 14336 + wm * 4096 + l16;
    aw[1] = aw[0] + 65536; aw[2] = aw[0] + 131072; ax[1] = ax[0] + 65536; ax[2] = ax[0] + 131072;
  }
  unsigned ca = l0 + CSO + wn * 448 + g * 16, sv = wm * 4096 + (g >> 1) * 512 + (lane & 15) * 32 + (g & 1) * 16;
  asm volatile("" : "+v"(aw[0]), "+v"(aw[1]), "+v"(aw[2]), "+v"(ax[0]), "+v"(ax[1]), "+v"(ax[2]), "+v"(ca), "+v"(sv));
  const int p0 = 14 * cg + 2 * (cg & 1), pq = p0 + 4 * wn, pp = p0 + 8 + 2 * wn, p1 = 14 * cg + ((cg & 1) ? 0 : 12) + wn;
  const unsigned mb = (unsigned)OFF_MB + tt0 * 55296;
  const unsigned mq = mb + (pq >> 2) * 1024, mp = mb + (pp >> 2) * 1024 + (pp & 3) * 4, m1 = mb + (p1 >> 2) * 1024 + (p1 & 3) * 4;
  unsigned vq = pq < 216 ? l16 : 0x80000000u, vp = pp < 216 ? l16 : 0x80000000u;
  asm volatile("" : "+v"(vq), "+v"(vp));
  auto rdw = [&](auto I) {
    constexpr int n = I.v, kb = n < 112 ? n / 7 : 16 + (n & 3), i = n < 112 ? n % 7 : (n - 112) / 4;
    constexpr int o = (kb % 7) * SLOT + i * 1024;
    wf[n % 3] = ldsr<s16x8>(aw[o >> 16] + (o & 65535));
  };
  auto rdx = [&](auto K, auto T) {
    constexpr int o = (K.v % 7) * SLOT + T.v * 1024;
    xf[K.v & 3][T.v] = ldsr<s16x8>(ax[o >> 16] + (o & 65535));
  };
  asm volatile("global_load_dwordx4 %0, %4, %5\n\tglobal_load_dwordx4 %1, %4, %5 offset:1024\n\t"
               "global_load_dwordx4 %2, %4, %5 offset:2048\n\tglobal_load_dwordx4 %3, %4, %5 offset:3072"
               : "=&v"(rs[0]), "=&v"(rs[1]), "=&v"(rs[2]), "=&v"(rs[3]) : "v"(sv), "s"(ws + (OFF_ST + tb * 8192)) : "memory");
  waitv<4>();
  bar();
  STAMP(4, 1);
  constexpr auto idle = [](int q) { return (q & 3) != 0 && (q % 28 < 18 || q % 28 > 23); };
  constexpr auto nidle = [idle](int b, int q) { int n = 0; for (int i = b; i < q; ++i) n += idle(i); return q < b ? -1 : n; };
  rdw(IC<0>{}); rdx(IC<0>{}, IC<0>{}); rdx(IC<0>{}, IC<1>{}); rdx(IC<0>{}, IC<2>{}); rdx(IC<0>{}, IC<3>{}); rdw(IC<1>{});
  PIN();
  sfor<0, 448>([&](auto I) {
    constexpr int q = I.v, kb = q / 28, r = q % 28, n = q / 4, t = q & 3;
    if constexpr (kb == 0) mfma16_new(acc[r], wf[n % 3], xf[kb & 3][t]);
    else mfma16_acc(acc[r], wf[n % 3], xf[kb & 3][t]);
    PADHERE("k_gu");
    auto add = [](float& x, float y) { asm volatile("v_add_f32 %0, %0, %1" : "+v"(x) : "v"(y)); };
    auto mov = [](float& x, float y) { asm volatile("v_mov_b32 %0, %1" : "=&v"(x) : "v"(y)); };
    auto x32 = [](float& x, float& y) { asm volatile("v_permlane32_swap_b32 %0, %1" : "+v"(x), "+v"(y)); };
    auto x16 = [](float& x, float& y) { asm volatile("v_permlane16_swap_b32 %0, %1" : "+v"(x), "+v"(y)); };
    auto sum = [](float& x, float y, float z) { asm volatile("v_add_f32 %0, %1, %2" : "=&v"(x) : "v"(y), "v"(z)); };
    if constexpr (q == QS) asm volatile("s_waitcnt vmcnt(0)" : "+v"(rs[0]), "+v"(rs[1]), "+v"(rs[2]), "+v"(rs[3]));
    if constexpr (idle(q)) {
      constexpr int z = nidle(QS, q), y = nidle(394, q);
      if constexpr (z >= 0 && z < 4) sum(nmean[(z & 1) * 2 + z / 2], rs[(z & 1) * 2 + z / 2][0], rs[(z & 1) * 2 + z / 2][2]);
      if constexpr (z == 4) sum(vr[0], rs[0][1], rs[0][3]);
      if constexpr (z == 5) sum(vr[2], rs[2][1], rs[2][3]);
      if constexpr (z == 6) x32(nmean[0], nmean[2]);
      if constexpr (z == 7) sum(vr[1], rs[1][1], rs[1][3]);
      if constexpr (z == 8) x32(nmean[1], nmean[3]);
      if constexpr (z == 9) sum(vr[3], rs[3][1], rs[3][3]);
      if constexpr (z == 10) add(nmean[0], nmean[2]);
      if constexpr (z == 11) x32(vr[0], vr[2]);
      if constexpr (z == 12) add(nmean[1], nmean[3]);
      if constexpr (z == 13) x32(vr[1], vr[3]);
      if constexpr (z == 14) add(vr[0], vr[2]);
      if constexpr (z == 15) x16(nmean[0], nmean[1]);
      if constexpr (z == 16) add(vr[1], vr[3]);
      if constexpr (z == 17) add(nmean[0], nmean[1]);
      if constexpr (z == 18) asm volatile("v_mul_f32 %0, 0xbacccccd, %0" : "+v"(nmean[0]));
      if constexpr (z == 19) x16(vr[0], vr[1]);
      if constexpr (z == 20) mov(nmean[2], nmean[0]);
      if constexpr (z == 21) add(vr[0], vr[1]);
      if constexpr (z == 22) asm volatile("v_mul_f32 %0, 0x3acccccd, %0" : "+v"(vr[0]));
      if constexpr (z == 23) asm volatile("v_fma_f32 %0, -%1, %1, %0" : "+v"(vr[0]) : "v"(nmean[0]));
      if constexpr (z == 24) x32(nmean[0], nmean[2]);
      if constexpr (z == 25) asm volatile("v_max_f32 %0, 0, %0" : "+v"(vr[0]));
      if constexpr (z == 26) mov(nmean[1], nmean[0]);
      if constexpr (z == 27) mov(nmean[3], nmean[2]);
      if constexpr (z == 28) asm volatile("v_add_f32 %0, 0x3727c5ac, %0" : "+v"(vr[0]));
      if constexpr (z == 29) x16(nmean[0], nmean[1]);
      if constexpr (z == 30) asm volatile("v_rsq_f32 %0, %1" : "=&v"(nk[0]) : "v"(vr[0]));
      if constexpr (z == 31) x16(nmean[2], nmean[3]);
      if constexpr (z == 32) asm volatile("v_mul_f32 %0, 0xbfb8aa3b, %0" : "+v"(nk[0]));
      float* d = (y & 1) ? nk : vr;
      if constexpr (y >= 0 && y < 2) mov(d[2], d[0]);
      if constexpr (y >= 2 && y < 4) x32(d[0], d[2]);
      if constexpr (y >= 4 && y < 8) mov(d[(y & 2) + 1], d[y & 2]);
      if constexpr (y >= 8 && y < 12) x16(d[y & 2], d[(y & 2) + 1]);
    }
    if constexpr (r == (kb == 11 ? 10 : kb == 13 ? 2 : 18) && (kb == 0 || (kb & 1))) bar();
    if constexpr (t == 0) rdw(IC<n + 2>{});
    if constexpr (r == 19 || (r >= 21 && r <= 23)) rdx(IC<kb + 1>{}, IC<(r == 19 ? 0 : r - 20)>{});
    if constexpr (kb == 15 && r >= 25) rdx(IC<17>{}, IC<r - 25>{});
    if constexpr (q >= Q0 - 7 && q < Q0 + 96 && (q - Q0 + 7) % 16 == 0) {
      constexpr int c = (q - Q0 + 7) / 16;
      c4[c & 1] = ldsr<f32x4>(ca + ((4 * c + 5) % 7) * 64);
    }
    if constexpr (q >= Q0 && q < Q0 + 112) {
      constexpr int c = (q - Q0) / 16, a = ((4 * c + 5) % 7) * 4 + ((q >> 2) & 3);
      float y = acc[a][t];
      asm volatile("v_fmac_f32 %0, %1, %2" : "+v"(y) : "v"(nmean[a & 3]), "v"(c4[c & 1][t]));
      acc[a][t] = y;
    }
    PIN();
  });
  STAMP(4, 2);
  unsigned o[4][7];
  float u1 = 0.f, r1 = 0.f, y0 = 0.f;
  sfor<0, 28>([&](auto I) {
    constexpr int z = I.v, i = z / 4, u = z & 3, n = 112 + z;
    mfma16_acc(acc[4 * i], wf[n % 3], xf[u][0]);
    if constexpr (z == 0) { rdx(IC<17>{}, IC<3>{}); bar(); }
    if constexpr (z == 10) bar();
    PADHERE("k_gu");
    if constexpr (n + 2 < 140) rdw(IC<n + 2>{});
    PIN();
    if constexpr (i == 0) {
      sfor<1, 4>([&](auto T) {
        mfma16_acc(acc[T.v], wf[n % 3], xf[u][T.v]);
        constexpr int x = 3 * z + T.v - 1;
        if constexpr (x < 8) rdx(IC<18 + x / 4>{}, IC<(x & 3)>{});
        PIN();
      });
    } else {
      const f32x4 y = acc[4 * (i - 1) + u];
      float e0, e1, u0; unsigned ov;
      asm volatile("v_mul_f32 %[u1], %[u1], %[r1]\n\tv_mul_f32 %[e0], %[y0], %[nk]\n\tv_mul_f32 %[e1], %[y1], %[nk]\n\t"
                   "v_cvt_pk_bf16_f32 %[ov], %[yp], %[u1]\n\tv_exp_f32 %[e0], %[e0]\n\t"
                   "v_mfma_f32_16x16x32_bf16 %[c1], %[a], %[b1], %[c1]\n\t"
                   "v_exp_f32 %[e1], %[e1]\n\tv_mul_f32 %[u0], %[y0], %[y2]\n\tv_fma_f32 %[e0], %[e0], %[vr], %[vr]\n\t"
                   "v_mfma_f32_16x16x32_bf16 %[c2], %[a], %[b2], %[c2]\n\t"
                   "v_rcp_f32 %[e0], %[e0]\n\tv_fma_f32 %[e1], %[e1], %[vr], %[vr]\n\tv_mul_f32 %[u1], %[y1], %[y3]\n\t"
                   "v_mfma_f32_16x16x32_bf16 %[c3], %[a], %[b3], %[c3]\n\t"
                   "v_rcp_f32 %[r1], %[e1]\n\tv_mul_f32 %[yp], %[u0], %[e0]"
                   : [u1] "+v"(u1), [r1] "+v"(r1), [yp] "+v"(y0), [e0] "=&v"(e0), [e1] "=&v"(e1), [u0] "=&v"(u0), [ov] "=&v"(ov),
                     [c1] "+v"(acc[4 * i + 1]), [c2] "+v"(acc[4 * i + 2]), [c3] "+v"(acc[4 * i + 3])
                   : [y0] "v"(y[0]), [y1] "v"(y[1]), [y2] "v"(y[2]), [y3] "v"(y[3]), [nk] "v"(nk[u]), [vr] "v"(vr[u]),
                     [a] "v"(wf[n % 3]), [b1] "v"(xf[u][1]), [b2] "v"(xf[u][2]), [b3] "v"(xf[u][3]));
      if constexpr (z > 4) {
        constexpr int pi = (z - 1) / 4 - 1, pu = (z - 1) & 3;
        o[pu][pi] = ov;
        if constexpr (pi == 3) bst16<K4A>(u32x4{o[pu][0], o[pu][1], o[pu][2], o[pu][3]}, rw, vq, mq + pu * 55296);
        if constexpr (pi == 5) bst8<K4B>(u32x2{o[pu][4], o[pu][5]}, rw, vp, mp + pu * 55296);
      }
      PIN();
    }
  });
  asm volatile("s_nop 7" : "+v"(u1), "+v"(r1), "+v"(y0), "+v"(acc[24]), "+v"(acc[25]), "+v"(acc[26]), "+v"(acc[27]));
  bst8<K4B>(u32x2{o[3][4], pk2(y0, u1 * r1)}, rw, vp, mp + 3 * 55296);
#pragma unroll
  for (int t = 0; t < 4; ++t) {
    const f32x4 y = acc[24 + t];
    const float e0 = __builtin_amdgcn_exp2f(y[0] * nk[t]), e1 = __builtin_amdgcn_exp2f(y[1] * nk[t]);
    o[t][6] = pk2(y[0] * y[2] * __builtin_amdgcn_rcpf(e0 * vr[t] + vr[t]), y[1] * y[3] * __builtin_amdgcn_rcpf(e1 * vr[t] + vr[t]));
  }
#pragma unroll
  for (int t = 0; t < 4; ++t) __builtin_amdgcn_raw_buffer_store_b32(o[t][6], rw, l16, m1 + t * 55296, K4C);
  STAMP(4, 3);
  STAMPE(4, 4);
}

extern "C" __global__ PAGE __launch_bounds__(512) void k_down(char* ws, char* out) {
  gemm_body<2>(nullptr, ws, out);
}

extern "C" void kernel_launch(void* const* d_in, const int* in_sizes, int n_in, void* d_out, int out_size,
                              void* d_ws, size_t ws_size, hipStream_t stream) {
  const char* x = (const char*)d_in[0];
  char* ws = (char*)d_ws;
  k_qkv<<<256, 512, 0, stream>>>(x, (const char*)d_in[1], (const char*)d_in[2], (const char*)d_in[4], (const char*)d_in[5],
                                 (const char*)d_in[6], (const char*)d_out, ws);
  k_attn<<<256, 512, 0, stream>>>(ws, (const unsigned short*)d_in[2], (const unsigned short*)d_in[4],
                                  (const unsigned short*)d_in[5], (const unsigned short*)d_in[6]);
  k_oproj<<<256, 512, 0, stream>>>(x, ws);
  k_gu<<<256, 512, 0, stream>>>(ws);
  k_down<<<256, 512, 0, stream>>>(ws, (char*)d_out);
}
```
